```python
import torch

def _softmax(x, axis=-1):
    return torch.softmax(x, dim=axis)
def _gelu(x):
    return torch.nn.functional.gelu(x, approximate="tanh")
def _silu(x):
    return torch.nn.functional.silu(x)
def _sigmoid(x):
    return torch.sigmoid(x)
def _layernorm(x, axis=-1, eps=1e-5):
    m = x.mean(dim=axis, keepdim=True)
    v = x.var(dim=axis, keepdim=True, unbiased=False)
    return (x - m) / torch.sqrt(v + eps)
def _scatter_add(tgt, idx, src, axis=0):
    return tgt.clone().index_add_(axis, idx, src)
def _segment_sum(src, seg, n):
    out = torch.zeros((n,) + src.shape[1:], dtype=src.dtype)
    return out.index_add_(0, seg, src)
def _pad(x, pw):
    flat = tuple(v for pair in reversed(pw) for v in pair)
    return torch.nn.functional.pad(x, flat)

kernel_name = 'concat_heads__linear_kernel_hedgehog__ssm_conv_s4__prompt'

def setup_inputs():

    _w = (lambda a: (lambda t: t.to(torch.bfloat16) if t.is_floating_point() else t)(torch.as_tensor(a)))
    def _A_setup():
        B, S, D, H = 1, 2048, 512, 32
        Dh = D // H
        return {
            "x": _w((lambda *s: torch.randn(*s, dtype=torch.bfloat16) * 0.4)(B, S, D)),
            "wqkv": _w((lambda *s: torch.randn(*s, dtype=torch.bfloat16) * 0.4)(D, 3 * D)),
            "w_phi": _w((lambda *s: torch.randn(*s, dtype=torch.bfloat16) * 0.4)(H, Dh, Dh) * (Dh ** -0.5)),
            "wo": _w((lambda *s: torch.randn(*s, dtype=torch.bfloat16) * 0.05)(D, D)),
        }
    _A_in = _A_setup()

    def _B_setup():
        B, L, D, N = 1, 2048, 512, 128
        A_re = -(lambda _x: torch.exp(torch.as_tensor(_x)))((lambda *s: torch.randn(*s, dtype=torch.bfloat16) * 6.4)(D, N))
        A_im = torch.pi * (lambda *s: torch.randn(*s, dtype=torch.bfloat16) * 0.1)(D, N)
        B_re = (lambda *s: torch.randn(*s, dtype=torch.bfloat16) * 0.2)(D, N)
        B_im = (lambda *s: torch.randn(*s, dtype=torch.bfloat16) * 0.2)(D, N)
        C_re = (lambda *s: torch.randn(*s, dtype=torch.bfloat16) * 0.2)(D, N)
        C_im = (lambda *s: torch.randn(*s, dtype=torch.bfloat16) * 0.2)(D, N)
        log_dt = (lambda *s: torch.randn(*s, dtype=torch.bfloat16) * 0.1)(D) * 0.5 - 4.0
        Dskip = (lambda *s: torch.randn(*s, dtype=torch.bfloat16) * 0.1)(D)
        return {
            "u": _w((lambda *s: torch.randn(*s, dtype=torch.bfloat16) * 0.05)(B, L, D)),
            "A_re": _w(A_re),
            "A_im": _w(A_im),
            "B_re": _w(B_re),
            "B_im": _w(B_im),
            "C_re": _w(C_re),
            "C_im": _w(C_im),
            "log_dt": _w(log_dt),
            "Dskip": _w(Dskip),
        }
    _B_in = _B_setup()

    return {
        "A_x": _A_in["x"],
        "A_wqkv": _A_in["wqkv"],
        "A_w_phi": _A_in["w_phi"],
        "A_wo": _A_in["wo"],
        "B_u": _B_in["u"],
        "B_A_re": _B_in["A_re"],
        "B_A_im": _B_in["A_im"],
        "B_B_re": _B_in["B_re"],
        "B_B_im": _B_in["B_im"],
        "B_C_re": _B_in["C_re"],
        "B_C_im": _B_in["C_im"],
        "B_log_dt": _B_in["log_dt"],
        "B_Dskip": _B_in["Dskip"],
    }

def reference(A_x, A_wqkv, A_w_phi, A_wo, B_u, B_A_re, B_A_im, B_B_re, B_B_im, B_C_re, B_C_im, B_log_dt, B_Dskip):
    def _A_ref(x, wqkv, w_phi, wo):
        B, S, D = x.shape
        H = 32; Dh = D // H
        qkv = (lambda x, *d: x.permute(*d))((x @ wqkv).reshape(B, S, 3, H, Dh), 2, 0, 3, 1, 4)
        q, k, v = qkv[0], qkv[1], qkv[2]
        pq = torch.einsum("bhsd,hdf->bhsf", q, w_phi)
        pk = torch.einsum("bhsd,hdf->bhsf", k, w_phi)
        phi_q = torch.concatenate([_softmax(pq, axis=-1), _softmax(-pq, axis=-1)], axis=-1)
        phi_k = torch.concatenate([_softmax(pk, axis=-1), _softmax(-pk, axis=-1)], axis=-1)
        kv = torch.einsum("bhsf,bhsd->bhfd", phi_k, v)
        z  = torch.einsum("bhsf->bhf", phi_k)
        num = torch.einsum("bhqf,bhfd->bhqd", phi_q, kv)
        den = torch.einsum("bhqf,bhf->bhq", phi_q, z)[..., None] + 1e-6
        o = num / den
        return (lambda x, *d: x.permute(*d))(o, 0, 2, 1, 3).reshape(B, S, D) @ wo

    def _B_ref(u, A_re, A_im, B_re, B_im, C_re, C_im, log_dt, Dskip):
        Bsz, L, D = u.shape
        dt = torch.exp(log_dt)[:, None]
        A = A_re + 1j * A_im
        Bc = B_re + 1j * B_im
        Cc = C_re + 1j * C_im
        logdA = dt * A
        dB = dt * Bc
        l = (lambda x, dt: x.to(dt))(torch.arange(L), A_re.dtype)
        P = torch.exp(logdA[:, :, None] * l[None, None, :])
        K = torch.einsum("dn,dnl,dn->dl", Cc, P, dB).real
        L2 = 2 * L
        Kp = _pad(K, ((0, 0), (0, L)))
        up = (lambda x, *d: x.permute(*d))(u, 0, 2, 1)
        up = _pad(up, ((0, 0), (0, 0), (0, L)))
        Kf = (lambda x, axes=(-2,-1): torch.fft.rfft2(x.float(), dim=axes))(Kp[:, None, :], axes=(-2, -1))
        Uf = (lambda x, axes=(-2,-1): torch.fft.rfft2(x.float(), dim=axes))(up[:, :, None, :], axes=(-2, -1))
        Yf = Uf * Kf[None, :, :, :]
        y = (lambda x, s, axes=(-2,-1): torch.fft.irfft2(x, s=s, dim=axes).to(torch.bfloat16))(Yf, (1, L2), axes=(-2, -1))[:, :, 0, :L]
        y = (lambda x, *d: x.permute(*d))(y, 0, 2, 1) + u * Dskip[None, None, :]
        return y

    a = _A_ref(A_x, A_wqkv, A_w_phi, A_wo)
    b = _B_ref(B_u, B_A_re, B_A_im, B_B_re, B_B_im, B_C_re, B_C_im, B_log_dt, B_Dskip)
    return torch.concatenate([a, b], axis=-1)
```

```cpp
#include <hip/hip_runtime.h>
#include <hip/hip_bf16.h>

typedef unsigned int u32;
typedef unsigned long long u64;
typedef unsigned short u16;
typedef short s16x4 __attribute__((ext_vector_type(4)));
typedef short s16x8 __attribute__((ext_vector_type(8)));
typedef __bf16 bf16x2 __attribute__((ext_vector_type(2)));
typedef __bf16 bf16x8 __attribute__((ext_vector_type(8)));
typedef float f32x2 __attribute__((ext_vector_type(2)));
typedef float f32x4 __attribute__((ext_vector_type(4)));
typedef float f32x16 __attribute__((ext_vector_type(16)));
typedef u32 u32x2 __attribute__((ext_vector_type(2)));
typedef u32 u32x4 __attribute__((ext_vector_type(4)));
#define LAS __attribute__((address_space(3)))
#define GAS __attribute__((address_space(1)))
#define DI __device__ __forceinline__
typedef LAS char* lptr;

constexpr u32 WS_EPOCH = 0;
constexpr u32 WS_KVF = 4096;
constexpr u32 WS_OF = WS_KVF + 256 * 128;
constexpr u32 WS_UF = WS_OF + 1024 * 128;
constexpr u32 WS_YF = WS_UF + 1024 * 128;
constexpr u32 WS_KVP = 1u << 19;
constexpr u32 WS_OFR = 2u << 20;
constexpr u32 WS_UX = 4u << 20;
constexpr u32 WS_YT = 6u << 20;
constexpr u32 WS_END = 8u << 20;
constexpr int LDS_BYTES = 81920;
constexpr u32 SPIN_CAP = 1u << 16;

#define STAMP(k)

#define BAR() asm volatile("s_waitcnt lgkmcnt(0)\n\ts_barrier" ::: "memory")
#define WAITV(n) asm volatile("s_waitcnt vmcnt(" #n ")" ::: "memory")
#define MEMB() asm volatile("" ::: "memory")

DI u32 pk2(float a, float b) { f32x2 v = {a, b}; return __builtin_bit_cast(u32, __builtin_convertvector(v, bf16x2)); }
DI float bf_lo(u32 v) { return __uint_as_float(v << 16); }
DI float bf_hi(u32 v) { return __uint_as_float(v & 0xffff0000u); }
DI float bf1(u16 v) { return __uint_as_float((u32)v << 16); }
DI float rbf(float a) { return bf_lo(pk2(a, a)); }
DI u32x2 trd(lptr p) { return __builtin_bit_cast(u32x2, __builtin_amdgcn_ds_read_tr16_b64_v4i16((LAS s16x4*)p)); }
DI u32x4 trd2(lptr p0, lptr p1) { u32x2 a = trd(p0), b = trd(p1); return (u32x4){a.x, a.y, b.x, b.y}; }
DI u32x4 lds128(lptr p) { return *(LAS u32x4*)p; }
DI u32x2 lds64(lptr p) { return *(LAS u32x2*)p; }
DI f32x16 mfma32(u32x4 a, u32x4 b, f32x16 c) { return __builtin_amdgcn_mfma_f32_32x32x16_bf16(__builtin_bit_cast(bf16x8, a), __builtin_bit_cast(bf16x8, b), c, 0, 0, 0); }
DI f32x4 mfma16(u32x4 a, u32x4 b, f32x4 c) { return __builtin_amdgcn_mfma_f32_16x16x32_bf16(__builtin_bit_cast(bf16x8, a), __builtin_bit_cast(bf16x8, b), c, 0, 0, 0); }
DI void glds16(const char* g, lptr l) {
  const u32 d = __builtin_amdgcn_readfirstlane((u32)(size_t)l);
  u32 keep;
  asm volatile("s_mov_b32 %0, m0\n\ts_mov_b32 m0, %2\n\ts_nop 0\n\tglobal_load_lds_dwordx4 %1, off\n\ts_mov_b32 m0, %0" : "=&s"(keep) : "v"(g), "s"(d) : "memory");
}
DI u64 sload64(const char* p) { u64 v; asm volatile("s_load_dwordx2 %0, %1, 0x0\n\ts_waitcnt lgkmcnt(0)" : "=s"(v) : "s"(p) : "memory"); return v; }
#define PIN(x) asm volatile("" : "+v"(x))
DI u64 mk_tag(u64 e) { return (e + 0x632BE59BD9B4E019ull) * 0x9E3779B97F4A7C15ull; }
#define RSRC __amdgpu_buffer_rsrc_t
DI void st128c(RSRC r, u32 off, u32x4 v) { __builtin_amdgcn_raw_buffer_store_b128(v, r, off, 0, 16); }
DI u32x4 ld128c(RSRC r, u32 off) { return __builtin_amdgcn_raw_buffer_load_b128(r, off, 0, 16); }
DI u32x2 ld64c(RSRC r, u32 off) { return __builtin_amdgcn_raw_buffer_load_b64(r, off, 0, 16); }
DI void set_flag(char* ws, u32 off, u64 tag) { __hip_atomic_store((GAS u64*)(ws + off), tag, __ATOMIC_RELAXED, __HIP_MEMORY_SCOPE_AGENT); }
DI void wait_flags(char* ws, u32 off, u64 tag) {
#pragma unroll 1
  for (u32 spins = 0; spins < SPIN_CAP; ++spins) {
    MEMB();
    u64 f = __hip_atomic_load((GAS u64*)(ws + off), __ATOMIC_RELAXED, __HIP_MEMORY_SCOPE_AGENT);
    if (__all(f == tag)) break;
    __builtin_amdgcn_s_sleep(1);
  }
  MEMB();
}

DI void softmax16(const f32x16& p, u32x4& lo, u32x4& hi) {
  float m = fmaxf(fmaxf(fmaxf(p[0], p[1]), fmaxf(p[2], p[3])), fmaxf(fmaxf(p[4], p[5]), fmaxf(p[6], p[7])));
  m = fmaxf(m, fmaxf(fmaxf(fmaxf(p[8], p[9]), fmaxf(p[10], p[11])), fmaxf(fmaxf(p[12], p[13]), fmaxf(p[14], p[15]))));
  const float L2E = 1.4426950408889634f;
  const float mb = -m * L2E;
  float e[16];
#pragma unroll
  for (int i = 0; i < 16; ++i) e[i] = __builtin_amdgcn_exp2f(__builtin_fmaf(p[i], L2E, mb));
  float s = ((e[0] + e[1]) + (e[2] + e[3])) + ((e[4] + e[5]) + (e[6] + e[7]));
  s += ((e[8] + e[9]) + (e[10] + e[11])) + ((e[12] + e[13]) + (e[14] + e[15]));
  const float r = __builtin_amdgcn_rcpf(s);
  lo = (u32x4){pk2(e[0] * r, e[1] * r), pk2(e[2] * r, e[3] * r), pk2(e[4] * r, e[5] * r), pk2(e[6] * r, e[7] * r)};
  hi = (u32x4){pk2(e[8] * r, e[9] * r), pk2(e[10] * r, e[11] * r), pk2(e[12] * r, e[13] * r), pk2(e[14] * r, e[15] * r)};
}
DI u32x4 pack8(const f32x16& a, int o) { return (u32x4){pk2(a[o], a[o + 1]), pk2(a[o + 2], a[o + 3]), pk2(a[o + 4], a[o + 5]), pk2(a[o + 6], a[o + 7])}; }

DI void role_A(lptr sm, const char* x, const char* W, const char* wphi, const char* wo, char* out, char* ws) {
  const int b = blockIdx.x, xcd = b & 7, idx = b >> 3;
  const int p = (xcd >> 1) * 4 + (idx >> 3), j = (xcd & 1) * 8 + (idx & 7);
  const int tid = threadIdx.x, l = tid & 63, w = __builtin_amdgcn_readfirstlane(tid >> 6);
  const int hh = l >> 5, blk = (l >> 4) & 1, q4 = (l & 15) >> 2, pp = l & 3, r32 = l & 31;
  constexpr int XR = 0, WR = 49152, PHI = 73728, KVI = 74752;
  const RSRC rs = __builtin_amdgcn_make_buffer_rsrc(ws, 0, WS_END, 0x00020000);
  STAMP(0);
  const int r0 = 0;
  const char* xsrc = x + (size_t)(128 * p + 32 * w + (l >> 3)) * 1024;
  const int xs0 = ((l & 7) ^ ((l >> 4) & 7)) * 16, xs1 = ((l & 7) ^ ((4 + (l >> 4)) & 7)) * 16;
  const char* wsrc = W + (size_t)(l >> 2) * 3072 + j * 64 + (l & 3) * 16;
  auto issueX = [&](int c) {
    const int kc = (c + r0) & 7; lptr d = sm + XR + ((c % 3) * 4 + w) * 4096;
#pragma unroll
    for (int i = 0; i < 4; ++i) glds16(xsrc + i * 8192 + kc * 128 + ((i & 1) ? xs1 : xs0), d + i * 1024);
  };
  auto issueW = [&](int c) {
    const int kc = (c + r0) & 7; lptr d = sm + WR + (c % 3) * 8192 + w * 1024;
    const char* s = wsrc + (size_t)(64 * kc + 16 * w) * 3072;
    glds16(s + 1024, d); glds16(s + 2048, d + 4096);
  };
  auto issueQ = [&](int half) {
    const int kc = (4 * half + w + r0) & 7; lptr d = sm + XR + ((half ? 0 : 2) * 4 + w) * 4096;
#pragma unroll
    for (int i = 0; i < 4; ++i) glds16(wsrc + (size_t)(64 * kc + 16 * i) * 3072, d + i * 1024);
  };
  if (w == 0) glds16(wphi + j * 1024 + l * 16, sm + PHI);
  issueX(0); issueW(0); issueX(1); issueW(1); issueX(2);
  const u64 epoch = sload64(ws + WS_EPOCH);
  const u64 tag = mk_tag(epoch);

  const int wlane = (8 * hh + q4) * 64 + blk * 32 + pp * 8;
  const int xsw = (r32 >> 1) & 7;
  u32x4 xf[32];
  f32x16 ak = {}, av = {};
#pragma unroll
  for (int c = 0; c < 8; ++c) {
    if (c < 7) WAITV(10); else WAITV(8);
    BAR();
    if (c + 2 < 8) issueW(c + 2);
    lptr xs = sm + XR + ((c % 3) * 4 + w) * 4096 + r32 * 128;
    lptr wsl = sm + WR + (c % 3) * 8192 + wlane;
    u32x4 wk[4], wv[4];
#pragma unroll
    for (int ks = 0; ks < 4; ++ks) xf[c * 4 + ks] = lds128(xs + (((2 * ks + hh) ^ xsw) * 16));
#pragma unroll
    for (int ks = 0; ks < 4; ++ks) { wk[ks] = trd2(wsl + ks * 1024, wsl + ks * 1024 + 256); wv[ks] = trd2(wsl + 4096 + ks * 1024, wsl + 4096 + ks * 1024 + 256); }
    asm volatile("s_waitcnt lgkmcnt(0)" ::: "memory");
    if (c + 3 < 8) issueX(c + 3); else if (c == 5) issueQ(0); else if (c == 6) issueQ(1);
#pragma unroll
    for (int ks = 0; ks < 4; ++ks) { ak = mfma32(wk[ks], xf[c * 4 + ks], ak); av = mfma32(xf[c * 4 + ks], wv[ks], av); }
  }
  STAMP(1);
  u32x4 phif[2];
  {
    const u32 sg = ((l >> 2) & 1) ? 0x80008000u : 0u;
    lptr pb = sm + PHI + (4 * hh + q4) * 32 + (8 * blk + 4 * (pp >> 1)) * 2;
#pragma unroll
    for (int h = 0; h < 2; ++h) { u32x4 v = trd2(pb + h * 512, pb + h * 512 + 256); phif[h] = (u32x4){v.x ^ sg, v.y ^ sg, v.z ^ sg, v.w ^ sg}; }
  }
  {
    lptr tb = sm + XR + (4 + w) * 4096;
    const f32x16 zero = {};
#pragma unroll
    for (int h = 0; h < 2; ++h) {
      f32x16 pk = mfma32(phif[h], pack8(ak, 8 * h), zero);
      u32x4 lo, hi; softmax16(pk, lo, hi);
      *(LAS u32x4*)(tb + h * 2048 + r32 * 64 + hh * 32) = lo;
      *(LAS u32x4*)(tb + h * 2048 + r32 * 64 + hh * 32 + 16) = hi;
    }
    const u32x4 v0 = pack8(av, 0), v1 = pack8(av, 8);
    const u32 one = 0x3F803F80u;
    BAR();
    lptr tr = tb + (4 * hh + q4) * 64 + blk * 32 + pp * 8;
#pragma unroll
    for (int h = 0; h < 2; ++h) {
      const bool mine = (blk == h);
      const u32x4 b0 = mine ? v0 : (u32x4){one, one, one, one}, b1 = mine ? v1 : (u32x4){one, one, one, one};
      f32x16 kv = mfma32(trd2(tr + h * 2048, tr + h * 2048 + 512), b0, zero);
      kv = mfma32(trd2(tr + h * 2048 + 1024, tr + h * 2048 + 1536), b1, kv);
      const int cc = mine ? (l & 15) : 16;
      if (mine || (l & 15) == 0) {
        lptr rp = sm + WR + ((w * 2 + h) * 17 + cc) * 144 + hh * 16;
#pragma unroll
        for (int g = 0; g < 4; ++g) *(LAS f32x4*)(rp + g * 32) = (f32x4){kv[4 * g], kv[4 * g + 1], kv[4 * g + 2], kv[4 * g + 3]};
      }
    }
    BAR();
    if (tid < 136) {
      const int row = tid >> 2, f8 = tid & 3;
      lptr rp = sm + WR + row * 144 + f8 * 32;
      f32x4 a = *(LAS f32x4*)rp, bb = *(LAS f32x4*)(rp + 16);
#pragma unroll
      for (int ww = 1; ww < 4; ++ww) { a += *(LAS f32x4*)(rp + ww * 34 * 144); bb += *(LAS f32x4*)(rp + ww * 34 * 144 + 16); }
      const int h = row >= 17, c = row - 17 * h;
      const u32 off = (c < 16) ? (h * 1024 + c * 64) : (2048 + h * 64);
      st128c(rs, WS_KVP + (j * 16 + p) * 2176 + off + f8 * 16, (u32x4){pk2(a[0], a[1]), pk2(a[2], a[3]), pk2(bb[0], bb[1]), pk2(bb[2], bb[3])});
    }
    WAITV(0);
    BAR();
    if (tid == 0) set_flag(ws, WS_KVF + (j * 16 + p) * 128, tag);
  }
  STAMP(2);
  f32x16 aq = {};
#pragma unroll
  for (int c = 0; c < 8; ++c) {
    lptr wsl = sm + XR + (((c < 4) ? 2 : 0) * 4 + (c & 3)) * 4096 + wlane;
#pragma unroll
    for (int ks = 0; ks < 4; ++ks) aq = mfma32(trd2(wsl + ks * 1024, wsl + ks * 1024 + 256), xf[c * 4 + ks], aq);
  }
  BAR();
  const int rt = xcd * 4 + (idx >> 3), ct = idx & 7;
  {
    const char* s = wo + (size_t)(128 * w + ((l >> 2) & 7)) * 1024 + ct * 128 + (l >> 5) * 64 + (l & 3) * 16;
#pragma unroll
    for (int i = 0; i < 16; ++i) glds16(s + i * 8192, sm + (16 * w + i) * 1024);
  }
  u32x4 pq[2][2];
  {
    const f32x16 zero = {};
#pragma unroll
    for (int h = 0; h < 2; ++h) { f32x16 t = mfma32(phif[h], pack8(aq, 8 * h), zero); softmax16(t, pq[h][0], pq[h][1]); PIN(pq[h][0]); PIN(pq[h][1]); }
  }
  STAMP(3);
  wait_flags(ws, WS_KVF + (j * 16 + (l & 15)) * 128, tag);
  STAMP(4);
  {
    const u32 base = WS_KVP + j * 16 * 2176;
    u32x4 v[8];
#pragma unroll
    for (int i = 0; i < 8; ++i) v[i] = ld128c(rs, base + (2 * i + (tid & 1)) * 2176 + (tid >> 1) * 16);
    const u32x2 zv = ld64c(rs, base + ((tid >> 3) & 15) * 2176 + 2048 + (tid >> 7) * 64 + (tid & 7) * 8);
    float a[8] = {};
#pragma unroll
    for (int i = 0; i < 8; ++i)
#pragma unroll
      for (int d = 0; d < 4; ++d) { a[2 * d] += bf_lo(v[i][d]); a[2 * d + 1] += bf_hi(v[i][d]); }
#pragma unroll
    for (int d = 0; d < 8; ++d) a[d] += __shfl_xor(a[d], 1);
    const int c = tid >> 1;
    const u32x2 o = (tid & 1) ? (u32x2){pk2(a[4], a[5]), pk2(a[6], a[7])} : (u32x2){pk2(a[0], a[1]), pk2(a[2], a[3])};
    *(LAS u32x2*)(sm + KVI + (c >> 6) * 2048 + (c & 63) * 16 + (tid & 1) * 8) = o;
    *(LAS u32x2*)(sm + KVI + (tid >> 7) * 2048 + 1024 + (tid & 127) * 8) = zv;
  }
  BAR();
  STAMP(5);
  {
    const f32x16 zero = {};
#pragma unroll
    for (int h = 0; h < 2; ++h) {
      lptr ap = sm + KVI + h * 2048 + r32 * 64 + hh * 32;
      f32x16 t = mfma32(lds128(ap), pq[h][0], zero);
      t = mfma32(lds128(ap + 16), pq[h][1], t);
      float den = ((t[8] + t[9]) + (t[10] + t[11])) + ((t[12] + t[13]) + (t[14] + t[15]));
      den += __shfl_xor(den, 32);
      const float r = __builtin_amdgcn_rcpf(den + 1e-6f);
      st128c(rs, WS_OFR + ((4 * p + w) * 32 + 2 * j + h) * 1024 + l * 16,
             (u32x4){pk2(t[0] * r, t[1] * r), pk2(t[2] * r, t[3] * r), pk2(t[4] * r, t[5] * r), pk2(t[6] * r, t[7] * r)});
    }
    WAITV(0);
    if (l == 0) set_flag(ws, WS_OF + ((4 * p + w) * 16 + j) * 128, tag);
  }
  STAMP(6);
  BAR();
  const int sh = w & 1, kh = w >> 1, st = 2 * rt + sh;
  wait_flags(ws, WS_OF + (st * 16 + 8 * kh + (l & 7)) * 128, tag);
  STAMP(7);
  f32x16 acc0 = {}, acc1 = {};
  {
    u32x4 of[16];
#pragma unroll
    for (int i = 0; i < 16; ++i) of[i] = ld128c(rs, WS_OFR + (st * 32 + 16 * kh + i) * 1024 + l * 16);
    MEMB(); __builtin_amdgcn_sched_barrier(0);
    lptr wb = sm + kh * 32768 + (4 * hh + q4) * 64 + blk * 32 + pp * 8;
#pragma unroll
    for (int i = 0; i < 16; ++i) {
      acc0 = mfma32(trd2(wb + i * 2048, wb + i * 2048 + 1024), of[i], acc0);
      acc1 = mfma32(trd2(wb + i * 2048 + 512, wb + i * 2048 + 1536), of[i], acc1);
    }
  }
  BAR();
  STAMP(8);
  {
    lptr rp = sm + kh * 17408 + (32 * sh + r32) * 272 + hh * 16;
#pragma unroll
    for (int g = 0; g < 4; ++g) {
      *(LAS f32x4*)(rp + g * 32) = (f32x4){acc0[4 * g], acc0[4 * g + 1], acc0[4 * g + 2], acc0[4 * g + 3]};
      *(LAS f32x4*)(rp + 128 + g * 32) = (f32x4){acc1[4 * g], acc1[4 * g + 1], acc1[4 * g + 2], acc1[4 * g + 3]};
    }
    BAR();
#pragma unroll
    for (int i = 0; i < 2; ++i) {
      const int row = i * 32 + (tid >> 3), oc = tid & 7;
      lptr sp = sm + row * 272 + oc * 32;
      const f32x4 a = *(LAS f32x4*)sp + *(LAS f32x4*)(sp + 17408), bb = *(LAS f32x4*)(sp + 16) + *(LAS f32x4*)(sp + 17408 + 16);
      *(u32x4*)(out + (size_t)(64 * rt + row) * 2048 + ct * 128 + oc * 16) = (u32x4){pk2(a[0], a[1]), pk2(a[2], a[3]), pk2(bb[0], bb[1]), pk2(bb[2], bb[3])};
    }
  }
  if (b == 0 && tid == 0) set_flag(ws, WS_EPOCH, epoch + 1);
  STAMP(9);
}

struct cf { float re, im; };
DI cf cmul(cf a, cf b) { return {__builtin_fmaf(a.re, b.re, -a.im * b.im), __builtin_fmaf(a.re, b.im, a.im * b.re)}; }
DI cf cexp_s(float a2, float brev, float s) {
  const float m = __builtin_amdgcn_exp2f(a2 * s);
  const float th = __builtin_amdgcn_fractf(brev * s);
  return {m * __builtin_amdgcn_cosf(th), m * __builtin_amdgcn_sinf(th)};
}

DI void role_B(lptr sm, const char* u, const u16* Are, const u16* Aim, const u16* Bre, const u16* Bim, const u16* Cre, const u16* Cim,
               const u16* ldt, const char* Dsk, char* out, char* ws) {
  const int b = blockIdx.x - 256, gam = b & 7, idx = b >> 3;
  const int tid = threadIdx.x, l = tid & 63, w = __builtin_amdgcn_readfirstlane(tid >> 6);
  const int g4 = l >> 4, i16 = l & 15, q4 = i16 >> 2, pp = l & 3;
  constexpr int TROW = 528, TCH = 57 * TROW;
  constexpr int TAB = 0;
  constexpr int ZST = 8224;
  constexpr int UT = 60416;
  constexpr int KC = 60416;
  constexpr int KL = 72704;
  constexpr int KST = 4128;
  constexpr int UL = 0;
  (void)UL;
  const RSRC rs = __builtin_amdgcn_make_buffer_rsrc(ws, 0, WS_END, 0x00020000);
  STAMP(0);
  u32x4 ur[2];
  const char* urow = u + (size_t)(64 * idx + 16 * w + i16) * 1024 + gam * 128 + g4 * 16;
  ur[0] = *(const u32x4*)urow; ur[1] = *(const u32x4*)(urow + 64);
  const int ch = 64 * gam + 2 * idx + (tid >> 7), pi = ch * 128 + (tid & 127);
  const float are = bf1(Are[pi]), aim = bf1(Aim[pi]), bre = bf1(Bre[pi]), bim = bf1(Bim[pi]), cre = bf1(Cre[pi]), cim = bf1(Cim[pi]);
  const float ld = bf1(ldt[ch]);
  const u64 epoch = sload64(ws + WS_EPOCH + 128 * (1 + gam));
  const u64 tag = mk_tag(epoch);
  *(LAS u32x4*)(sm + UT + (16 * w + i16) * 144 + g4 * 16) = ur[0];
  *(LAS u32x4*)(sm + UT + (16 * w + i16) * 144 + 64 + g4 * 16) = ur[1];
  BAR();
#pragma unroll
  for (int e = 0; e < 2; ++e) {
    const int rho = 2 * e + (g4 >> 1), i0 = 8 * (g4 & 1);
    lptr a = sm + UT + (4 * (i0 + q4) + rho) * 144 + w * 32 + pp * 8;
    st128c(rs, WS_UX + (64 * gam + 16 * w + i16) * 4096 + idx * 128 + (4 * e + g4) * 16, trd2(a, a + 16 * 144));
  }
  WAITV(0);
  if (l == 0) set_flag(ws, WS_UF + ((gam * 4 + w) * 32 + idx) * 128, tag);
  STAMP(1);
  {
    const float dt = rbf(__expf(ld));
    const float a2 = (dt * are) * 1.4426950408889634f, brev = (dt * aim) * 0.15915494309189535f;
    const cf c = cmul({cre, cim}, {dt * bre, dt * bim});
    const cf w1 = cexp_s(a2, brev, 1.f), w8 = cexp_s(a2, brev, 8.f), w128 = cexp_s(a2, brev, 128.f);
    lptr tp = sm + TAB + (tid >> 7) * TCH + (tid & 127) * 4;
    auto putA = [&](int row, cf v) { *(LAS u32*)(tp + row * TROW) = pk2(v.re, v.im); };
    auto putB = [&](int row, cf v) { *(LAS u32*)(tp + (16 + row) * TROW) = pk2(v.re, -v.im); };
    cf e[16];
    e[0] = {1.f, 0.f}; e[1] = w8; e[2] = cmul(w8, w8); e[3] = cmul(e[2], w8); e[4] = cmul(e[2], e[2]);
#pragma unroll
    for (int i = 5; i < 8; ++i) e[i] = cmul(e[4], e[i - 4]);
    e[8] = cmul(e[4], e[4]);
#pragma unroll
    for (int i = 9; i < 16; ++i) e[i] = cmul(e[8], e[i - 8]);
#pragma unroll
    for (int i = 0; i < 16; ++i) putA(i, e[i]);
    cf h[17];
    const cf w256 = cmul(w128, w128), w512 = cmul(w256, w256);
    h[0] = c; h[1] = cmul(c, w128); h[2] = cmul(c, w256); h[3] = cmul(h[1], w256);
#pragma unroll
    for (int a = 4; a < 17; ++a) h[a] = cmul(h[a - 4], w512);
    const cf w2 = cmul(w1, w1), w4 = cmul(w2, w2);
#pragma unroll
    for (int a = 0; a < 2; ++a) {
      cf t[8]; t[0] = h[a]; t[1] = cmul(t[0], w1); t[2] = cmul(t[0], w2); t[3] = cmul(t[1], w2);
#pragma unroll
      for (int k = 4; k < 8; ++k) t[k] = cmul(t[k - 4], w4);
#pragma unroll
      for (int k = 0; k < 8; ++k) putB(8 * a + k, t[k]);
    }
#pragma unroll
    for (int a = 0; a < 2; ++a) {
      const cf t0 = h[2 + a], t1 = cmul(t0, w2);
      putB(16 + 4 * a, t0); putB(17 + 4 * a, t1); putB(18 + 4 * a, cmul(t0, w4)); putB(19 + 4 * a, cmul(t1, w4));
    }
#pragma unroll
    for (int a = 0; a < 4; ++a) { putB(24 + 2 * a, h[4 + a]); putB(25 + 2 * a, cmul(h[4 + a], w4)); }
#pragma unroll
    for (int a = 0; a < 9; ++a) putB(32 + a, h[8 + a]);
  }
  BAR();
  STAMP(2);
  {
    const int cw = w >> 1, kh = w & 1;
    lptr tb = sm + TAB + cw * TCH + kh * 256 + g4 * 16;
    f32x4 d0 = {}, d1 = {}, d2 = {};
    const int r2 = 48 + (i16 < 8 ? i16 : 8);
#pragma unroll
    for (int ks = 0; ks < 4; ++ks) {
      const u32x4 a = lds128(tb + i16 * TROW + ks * 64);
      d0 = mfma16(a, lds128(tb + (16 + i16) * TROW + ks * 64), d0);
      d1 = mfma16(a, lds128(tb + (32 + i16) * TROW + ks * 64), d1);
      d2 = mfma16(a, lds128(tb + r2 * TROW + ks * 64), d2);
    }
    lptr kc = sm + KC + w * 3072 + i16 * 64 + g4 * 16;
    *(LAS f32x4*)kc = d0; *(LAS f32x4*)(kc + 1024) = d1; *(LAS f32x4*)(kc + 2048) = d2;
  }
  BAR();
  {
    int ci[8];
#pragma unroll
    for (int k = 0; k < 8; ++k) {
      const int x = (int)rbf((float)(8 * tid + k));
      const int row = (x >> 3) & 15, a = x >> 7;
      int col;
      if (x < 256) col = 8 * a + (x & 7);
      else if (x < 512) col = 16 + 4 * (a - 2) + ((x & 7) >> 1);
      else if (x < 1024) col = 24 + 2 * (a - 4) + ((x & 7) >> 2);
      else col = 32 + (a - 8);
      ci[k] = (col * 16 + row) * 4;
    }
#pragma unroll
    for (int c = 0; c < 2; ++c) {
      float v[8];
#pragma unroll
      for (int k = 0; k < 8; ++k) v[k] = *(LAS float*)(sm + KC + c * 6144 + ci[k]) + *(LAS float*)(sm + KC + c * 6144 + 3072 + ci[k]);
      *(LAS u32x4*)(sm + KL + c * KST + 32 + tid * 16) = (u32x4){pk2(v[0], v[1]), pk2(v[2], v[3]), pk2(v[4], v[5]), pk2(v[6], v[7])};
    }
    if (tid < 4) *(LAS u32x4*)(sm + KL + (tid >> 1) * KST + (tid & 1) * 16) = (u32x4){0, 0, 0, 0};
  }
  STAMP(3);
  constexpr int UP = 0, UST = 1152;
  if (tid < 64) *(LAS u32x4*)(sm + UP + (tid >> 3) * UST + (tid & 7) * 16) = (u32x4){0, 0, 0, 0};
  wait_flags(ws, WS_UF + ((gam * 4 + (idx >> 3)) * 32 + 8 * w + (l & 7)) * 128, tag);
  STAMP(4);
  {
    u32x4 v[2];
#pragma unroll
    for (int e = 0; e < 2; ++e) v[e] = ld128c(rs, WS_UX + (64 * gam + 2 * idx + e) * 4096 + tid * 16);
#pragma unroll
    for (int e = 0; e < 2; ++e) *(LAS u32x4*)(sm + UP + (e * 4 + ((tid >> 1) & 3)) * UST + 128 + (tid >> 3) * 32 + (tid & 1) * 16) = v[e];
  }
  BAR();
  if (idx == 0 && tid == 0) set_flag(ws, WS_EPOCH + 128 * (1 + gam), epoch + 1);
  STAMP(5);
  f32x4 acc[2][8];
#pragma unroll
  for (int c = 0; c < 2; ++c) {
    u32x4 af[16], bfr[16];
    lptr ka = sm + KL + c * KST + 32 + (4 * pp - 4 * (8 * g4 + q4) + 112) * 2;
    lptr ub = sm + UP + (c * 4 + w) * UST + 128 + (4 * i16 + 8 * g4 - 28 - 32) * 2;
#pragma unroll
    for (int m = 0; m < 16; ++m) {
      af[m] = trd2(ka + m * 256, ka + m * 256 - 32);
      const u32x2 x0 = lds64(ub + m * 64), x1 = lds64(ub + m * 64 + 8);
      bfr[m] = (u32x4){x0.x, x0.y, x1.x, x1.y};
    }
#pragma unroll
    for (int t = 0; t < 8; ++t) {
      f32x4 a = {};
#pragma unroll
      for (int m = 0; m <= 2 * t + 1; ++m) a = mfma16(bfr[2 * t - m + 1], af[m], a);
      acc[c][t] = a;
    }
  }
  BAR();
  STAMP(6);
#pragma unroll
  for (int c = 0; c < 2; ++c) {
    lptr zp = sm + (c * 4 + w) * ZST + (64 * g4 + i16 + w) * 4;
#pragma unroll
    for (int t = 0; t < 8; ++t)
#pragma unroll
      for (int i = 0; i < 4; ++i) *(LAS float*)(zp + (256 * t + 16 * i) * 4) = acc[c][t][i];
    if (l < w) *(LAS float*)(sm + (c * 4 + w) * ZST + l * 4) = 0.f;
  }
  BAR();
#pragma unroll
  for (int c = 0; c < 2; ++c) {
    lptr zp = sm + c * 4 * ZST + tid * 32;
    f32x4 a = *(LAS f32x4*)zp, bb = *(LAS f32x4*)(zp + 16);
#pragma unroll
    for (int r = 1; r < 4; ++r) { a += *(LAS f32x4*)(zp + r * ZST); bb += *(LAS f32x4*)(zp + r * ZST + 16); }
    st128c(rs, WS_YT + (64 * gam + 2 * idx + c) * 4096 + tid * 16, (u32x4){pk2(a[0], a[1]), pk2(a[2], a[3]), pk2(bb[0], bb[1]), pk2(bb[2], bb[3])});
  }
  WAITV(0);
  if (l == 0) set_flag(ws, WS_YF + ((gam * 4 + w) * 32 + idx) * 128, tag);
  STAMP(7);
  u32x4 dv[2];
  dv[0] = *(const u32x4*)(Dsk + gam * 128 + g4 * 16); dv[1] = *(const u32x4*)(Dsk + gam * 128 + 64 + g4 * 16);
  wait_flags(ws, WS_YF + ((gam * 4 + (idx >> 3)) * 32 + 8 * w + (l & 7)) * 128, tag);
  STAMP(8);
  constexpr int YS = 66048;
  {
    u32x4 v[2];
#pragma unroll
    for (int e = 0; e < 2; ++e) v[e] = ld128c(rs, WS_YT + (64 * gam + 16 * w + 8 * e + (l >> 3)) * 4096 + idx * 128 + (l & 7) * 16);
#pragma unroll
    for (int e = 0; e < 2; ++e) *(LAS u32x4*)(sm + YS + (16 * w + 8 * e + (l >> 3)) * 144 + (l & 7) * 16) = v[e];
  }
  BAR();
#pragma unroll
  for (int e = 0; e < 2; ++e) {
    lptr a = sm + YS + (8 * (4 * e + g4) + q4) * 144 + w * 32 + pp * 8;
    const u32x4 y = trd2(a, a + 4 * 144);
    u32x4 o;
#pragma unroll
    for (int d = 0; d < 4; ++d) {
      const u32 s = pk2(bf_lo(ur[e][d]) * bf_lo(dv[e][d]), bf_hi(ur[e][d]) * bf_hi(dv[e][d]));
      o[d] = pk2(bf_lo(y[d]) + bf_lo(s), bf_hi(y[d]) + bf_hi(s));
    }
    *(u32x4*)(out + (size_t)(64 * idx + 16 * w + i16) * 2048 + 1024 + gam * 128 + e * 64 + g4 * 16) = o;
  }
  STAMP(9);
}

__global__ __launch_bounds__(256, 2) void fused_kernel(const char* x, const char* W, const char* wphi, const char* wo, const char* u,
                                                       const u16* Are, const u16* Aim, const u16* Bre, const u16* Bim, const u16* Cre, const u16* Cim,
                                                       const u16* ldt, const char* Dsk, char* out, char* ws) {
  extern __shared__ __attribute__((aligned(16))) char smem[];
  lptr sm = (lptr)smem;
  if (blockIdx.x < 256) role_A(sm, x, W, wphi, wo, out, ws);
  else role_B(sm, u, Are, Aim, Bre, Bim, Cre, Cim, ldt, Dsk, out, ws);
}

extern "C" void kernel_launch(void* const* d_in, const int* in_sizes, int n_in, void* d_out, int out_size, void* d_ws, size_t ws_size,
                              hipStream_t stream) {
  (void)hipFuncSetAttribute((const void*)fused_kernel, hipFuncAttributeMaxDynamicSharedMemorySize, LDS_BYTES);
  fused_kernel<<<512, 256, LDS_BYTES, stream>>>((const char*)d_in[0], (const char*)d_in[1], (const char*)d_in[2], (const char*)d_in[3], (const char*)d_in[4],
                                                (const u16*)d_in[5], (const u16*)d_in[6], (const u16*)d_in[7], (const u16*)d_in[8], (const u16*)d_in[9], (const u16*)d_in[10],
                                                (const u16*)d_in[11], (const char*)d_in[12], (char*)d_out, (char*)d_ws);
}
```

```cpp
#include <hip/hip_runtime.h>
#include <hip/hip_bf16.h>

typedef unsigned int u32;
typedef unsigned long long u64;
typedef unsigned short u16;
typedef short s16x4 __attribute__((ext_vector_type(4)));
typedef short s16x8 __attribute__((ext_vector_type(8)));
typedef __bf16 bf16x2 __attribute__((ext_vector_type(2)));
typedef __bf16 bf16x8 __attribute__((ext_vector_type(8)));
typedef float f32x2 __attribute__((ext_vector_type(2)));
typedef float f32x4 __attribute__((ext_vector_type(4)));
typedef float f32x16 __attribute__((ext_vector_type(16)));
typedef u32 u32x2 __attribute__((ext_vector_type(2)));
typedef u32 u32x4 __attribute__((ext_vector_type(4)));
#define LAS __attribute__((address_space(3)))
#define GAS __attribute__((address_space(1)))
#if defined(__HIP_DEVICE_COMPILE__)
#define NOPK __attribute__((target("no-packed-fp32-ops")))
#else
#define NOPK
#endif
#define DI __device__ __forceinline__ NOPK
typedef LAS char* lptr;

constexpr u32 WS_EPOCH = 0;
constexpr u32 WS_KVF = 4096;
constexpr u32 WS_OF = WS_KVF + 1024 * 128;
constexpr u32 WS_UF = WS_OF + 1024 * 128;
constexpr u32 WS_YF = WS_UF + 1024 * 128;
constexpr u32 WS_KVP = 1u << 20;
constexpr u32 WS_OFR = 2u << 20;
constexpr u32 WS_UX = 4u << 20;
constexpr u32 WS_YT = 6u << 20;
constexpr u32 WS_END = 8u << 20;
constexpr int KVFC = 4;
constexpr int LDS_BYTES = 81920;
constexpr u32 SPIN_CAP = 1u << 16;

#define STAMPS_DECL
#define STAMP(k)
#define STAMPS_OUT()

#define BAR() asm volatile("s_waitcnt lgkmcnt(0)\n\ts_barrier" ::: "memory")
#define WAITV(n) asm volatile("s_waitcnt vmcnt(" #n ")" ::: "memory")
#define MEMB() asm volatile("" ::: "memory")
#define SCHEDB() __builtin_amdgcn_sched_barrier(0)

DI float u2f(u32 v) { return __builtin_bit_cast(float, v); }
DI u32 f2u(float v) { return __builtin_bit_cast(u32, v); }
DI bool all_lanes(bool c) { return __builtin_amdgcn_ballot_w64(!c) == 0; }
DI u32 pk2(float a, float b) { f32x2 v = {a, b}; return __builtin_bit_cast(u32, __builtin_convertvector(v, bf16x2)); }
DI float bf_lo(u32 v) { return u2f(v << 16); }
DI float bf_hi(u32 v) { return u2f(v & 0xffff0000u); }
DI float bf1(u16 v) { return u2f((u32)v << 16); }
DI float rbf(float a) { return bf_lo(pk2(a, a)); }
DI u32x2 trd(lptr p) { return __builtin_bit_cast(u32x2, __builtin_amdgcn_ds_read_tr16_b64_v4i16((LAS s16x4*)p)); }
DI u32x4 trd2(lptr p0, lptr p1) { u32x2 a = trd(p0), b = trd(p1); return (u32x4){a.x, a.y, b.x, b.y}; }
DI u32x4 lds128(lptr p) { return *(LAS u32x4*)p; }
DI u32x2 lds64(lptr p) { return *(volatile LAS u32x2*)p; }
DI f32x16 mfma32(u32x4 a, u32x4 b, f32x16 c) { return __builtin_amdgcn_mfma_f32_32x32x16_bf16(__builtin_bit_cast(bf16x8, a), __builtin_bit_cast(bf16x8, b), c, 0, 0, 0); }
DI f32x4 mfma16(u32x4 a, u32x4 b, f32x4 c) { return __builtin_amdgcn_mfma_f32_16x16x32_bf16(__builtin_bit_cast(bf16x8, a), __builtin_bit_cast(bf16x8, b), c, 0, 0, 0); }
DI u32x4 mk_srd(const void* p, u32 bytes) { const u64 a = (u64)p; return (u32x4){(u32)a, (u32)(a >> 32) & 0xffffu, bytes, 0x00020000u}; }
DI void dma1(u32 lds, u32 v0, u32x4 srd, u32 soff) {
  asm volatile("s_mov_b32 m0, %0\n\ts_nop 0\n\tbuffer_load_dwordx4 %1, %2, %3 offen lds" :: "s"(lds), "v"(v0), "s"(srd), "s"(soff) : "memory");
}
DI void dma2(u32 lds, u32 v0, u32x4 srd, u32 soff) {
  asm volatile("s_mov_b32 m0, %0\n\ts_nop 0\n\tbuffer_load_dwordx4 %1, %2, %3 offen lds\n\tbuffer_load_dwordx4 %1, %2, %3 offen offset:1024 lds"
               :: "s"(lds), "v"(v0), "s"(srd), "s"(soff) : "memory");
}
DI void dma4(u32 lds, u32 v0, u32 v1, u32 v2, u32 v3, u32x4 srd, u32 soff) {
  asm volatile("s_mov_b32 m0, %0\n\ts_nop 0\n\tbuffer_load_dwordx4 %1, %5, %6 offen lds\n\tbuffer_load_dwordx4 %2, %5, %6 offen offset:1024 lds\n\t"
               "buffer_load_dwordx4 %3, %5, %6 offen offset:2048 lds\n\tbuffer_load_dwordx4 %4, %5, %6 offen offset:3072 lds"
               :: "s"(lds), "v"(v0), "v"(v1), "v"(v2), "v"(v3), "s"(srd), "s"(soff) : "memory");
}
DI u64 sload64(const char* p) { u64 v; asm volatile("s_load_dwordx2 %0, %1, 0x0\n\ts_waitcnt lgkmcnt(0)" : "=s"(v) : "s"(p) : "memory"); return v; }
#define PIN(x) asm volatile("" : "+v"(x))
DI u64 mk_tag(u64 e) { return (e + 0x632BE59BD9B4E019ull) * 0x9E3779B97F4A7C15ull; }
#define RSRC __amdgpu_buffer_rsrc_t
DI void st128c(RSRC r, u32 off, u32x4 v) { __builtin_amdgcn_raw_buffer_store_b128(v, r, off, 0, 16); }
DI void st64c(RSRC r, u32 off, u32x2 v) { __builtin_amdgcn_raw_buffer_store_b64(v, r, off, 0, 16); }
DI u32x4 ld128c(RSRC r, u32 off) { return __builtin_amdgcn_raw_buffer_load_b128(r, off, 0, 16); }
DI u32x2 ld64c(RSRC r, u32 off) { return __builtin_amdgcn_raw_buffer_load_b64(r, off, 0, 16); }
DI void set_flag(char* ws, u32 off, u64 tag) { __hip_atomic_store((GAS u64*)(ws + off), tag, __ATOMIC_RELAXED, __HIP_MEMORY_SCOPE_AGENT); }
DI void wait_flags(char* ws, u32 off, u64 tag) {
#pragma unroll 1
  for (u32 spins = 0; spins < SPIN_CAP; ++spins) {
    MEMB();
    u64 f = __hip_atomic_load((GAS u64*)(ws + off), __ATOMIC_RELAXED, __HIP_MEMORY_SCOPE_AGENT);
    if (all_lanes(f == tag)) break;
    __builtin_amdgcn_s_sleep(1);
  }
  MEMB();
}

DI u32 uf_off(int blk, int w) { return WS_UF + (((blk & 7) * 4 + w) * 32 + (blk >> 3)) * 128; }

DI float max3f(float a, float b, float c) { float r; asm("v_max3_f32 %0, %1, %2, %3" : "=v"(r) : "v"(a), "v"(b), "v"(c)); return r; }
DI void softmax16(const f32x16& p, u32x4& lo, u32x4& hi) {
  const float m0 = __builtin_fmaxf(p[0], p[1]);
  const float m1 = __builtin_fmaxf(p[14], p[15]);
  float ma = max3f(m0, p[2], p[3]), mb_ = max3f(m1, p[4], p[5]);
  ma = max3f(ma, p[6], p[7]); mb_ = max3f(mb_, p[8], p[9]);
  ma = max3f(ma, p[10], p[11]); mb_ = max3f(mb_, p[12], p[13]);
  const float L2E = 1.4426950408889634f;
  const float mb = __builtin_fmaxf(ma, mb_) * -L2E;
  f32x2 e[8];
#pragma unroll
  for (int i = 0; i < 8; ++i) {
    const f32x2 t = __builtin_elementwise_fma((f32x2){p[2 * i], p[2 * i + 1]}, (f32x2){L2E, L2E}, (f32x2){mb, mb});
    e[i] = (f32x2){__builtin_amdgcn_exp2f(t.x), __builtin_amdgcn_exp2f(t.y)};
  }
  const f32x2 s2 = ((e[0] + e[1]) + (e[2] + e[3])) + ((e[4] + e[5]) + (e[6] + e[7]));
  const float r = __builtin_amdgcn_rcpf(s2.x + s2.y);
  u32 o[8];
#pragma unroll
  for (int i = 0; i < 8; ++i) { const f32x2 t = e[i] * (f32x2){r, r}; o[i] = pk2(t.x, t.y); }
  lo = (u32x4){o[0], o[1], o[2], o[3]}; hi = (u32x4){o[4], o[5], o[6], o[7]};
}
DI u32x4 pack8(const f32x16& a, int o) { return (u32x4){pk2(a[o], a[o + 1]), pk2(a[o + 2], a[o + 3]), pk2(a[o + 4], a[o + 5]), pk2(a[o + 6], a[o + 7])}; }

DI void role_A(lptr sm, const int b, const char* x, const char* W, const char* wphi, const char* wo, char* out, char* ws) {
  const int xcd = b & 7, idx = b >> 3;
  const int p = (xcd >> 1) * 4 + (idx >> 3), j = (xcd & 1) * 8 + (idx & 7);
  const int tid = threadIdx.x, l = tid & 63, w = __builtin_amdgcn_readfirstlane(tid >> 6);
  const int hh = l >> 5, blk = (l >> 4) & 1, i16 = l & 15, g4 = l >> 4, q4 = i16 >> 2, pp = l & 3, r32 = l & 31;
  constexpr int XR = 0, KR = 49152, QA = 61440, PHI = 77824, KVI = KR + 8192;
  const u32 lb = (u32)(size_t)sm;
  const RSRC rs = __builtin_amdgcn_make_buffer_rsrc(ws, 0, WS_END, 0x00020000);
  STAMPS_DECL; STAMP(0);
  const int r0 = (idx & 7) + 2 * (idx >> 3);
  const u32x4 sx = mk_srd(x, 2048 * 1024), sw = mk_srd(W, 512 * 3072);
  const u32 xv0 = (128 * p + 32 * w + (l >> 3)) * 1024 + ((l & 7) ^ g4) * 16;
  const u32 xv1 = (xv0 ^ 64) + 7168, xv2 = xv0 + 2 * 7168, xv3 = xv1 + 2 * 7168;
  const u32 wv0 = (l >> 2) * 3072 + j * 64 + pp * 16;
  auto issueX = [&](int c) NOPK { dma4(lb + XR + (c % 3) * 16384 + w * 4096, xv0, xv1, xv2, xv3, sx, ((c + r0) & 7) * 128); };
  auto issueK = [&](int c) NOPK { dma1(lb + KR + (c % 3) * 4096 + w * 1024, wv0, sw, (((c + r0) & 7) * 64 + 16 * w) * 3072 + 1024); };
  auto issueH = [&](int dst, int mat, int half) NOPK {
    dma4(lb + dst + w * 4096, wv0, wv0 + 48128, wv0 + 2 * 48128, wv0 + 3 * 48128, sw, ((4 * half + w + r0) & 7) * 64 * 3072 + mat * 1024);
  };
  issueX(0); issueK(0); issueX(1); issueK(1); issueX(2);
  if (w == 0) dma1(lb + PHI, l * 16, mk_srd(wphi, 32 * 512), j * 1024);
  const u64 epoch = sload64(ws + WS_EPOCH);
  const u64 tag = mk_tag(epoch);
  const int rt = xcd * 4 + (idx >> 3), ct = idx & 7;
  const int sh = w & 1, kh = w >> 1, st = 2 * rt + sh;

  const int wlane = (8 * hh + q4) * 64 + blk * 32 + pp * 8;
  const int xsw = (r32 >> 1) & 7;
  u32x4 xf[32], phif[2];
  f32x16 ak = {};
#pragma unroll
  for (int c = 0; c < 8; ++c) {
    if (c < 7) WAITV(9); else WAITV(8);
    BAR();
    if (c == 0) STAMP(1); if (c == 4) STAMP(2); if (c == 7) STAMP(3);
    lptr xs = sm + XR + (c % 3) * 16384 + w * 4096 + r32 * 128;
    lptr wsl = sm + KR + (c % 3) * 4096 + wlane;
    u32x4 wk[4];
#pragma unroll
    for (int ks = 0; ks < 4; ++ks) { xf[c * 4 + ks] = lds128(xs + (((2 * ks + hh) ^ xsw) * 16)); wk[ks] = trd2(wsl + ks * 1024, wsl + ks * 1024 + 256); }
    if (c + 2 < 8) issueK(c + 2);
    SCHEDB();
#pragma unroll
    for (int ks = 0; ks < 4; ++ks) ak = mfma32(wk[ks], xf[c * 4 + ks], ak);
    SCHEDB();
    if (c + 3 < 8) issueX(c + 3); else if (c == 5) issueH(XR + 32768, 2, 0); else if (c == 6) issueH(XR, 2, 1); else issueH(QA, 0, 0);
    if (c == 3) {
      const u32 sg = ((l >> 2) & 1) ? 0x80008000u : 0u;
      lptr pb = sm + PHI + (4 * hh + q4) * 32 + (8 * blk + 4 * (pp >> 1)) * 2;
#pragma unroll
      for (int h = 0; h < 2; ++h) { u32x4 v = trd2(pb + h * 512, pb + h * 512 + 256); phif[h] = (u32x4){v.x ^ sg, v.y ^ sg, v.z ^ sg, v.w ^ sg}; }
    }
    SCHEDB();
  }
  STAMP(4);
  {
    constexpr int PT = XR + 16384, VT = KR;
    const f32x16 zero = {};
    f32x16 pk[2], av = {};
    pk[0] = mfma32(phif[0], pack8(ak, 0), zero); pk[1] = mfma32(phif[1], pack8(ak, 8), zero);
    lptr tb = sm + PT + w * 4096 + l * 32;
#pragma unroll
    for (int h = 0; h < 2; ++h) {
      if (h == 0) WAITV(8); else { STAMP(5); WAITV(4); }
      BAR();
      if (h == 0) STAMP(6); else STAMP(7);
      SCHEDB();
      lptr wsl = sm + XR + (h ? 0 : 32768) + wlane;
#pragma unroll
      for (int i = 0; i < 16; ++i) av = mfma32(xf[16 * h + i], trd2(wsl + i * 1024, wsl + i * 1024 + 256), av);
      u32x4 lo, hi;
      softmax16(pk[h], lo, hi);
      *(LAS u32x4*)(tb + h * 2048) = lo; *(LAS u32x4*)(tb + h * 2048 + 16) = hi;
      __builtin_amdgcn_sched_group_barrier(0x100, 6, 0);
      __builtin_amdgcn_sched_group_barrier(0x002, 8, 0);
#pragma unroll
      for (int i = 0; i < 16; ++i) {
        __builtin_amdgcn_sched_group_barrier(0x008, 1, 0);
        __builtin_amdgcn_sched_group_barrier(0x100, 2, 0);
        __builtin_amdgcn_sched_group_barrier(0x402, 5, 0);
      }
      SCHEDB();
    }
    *(LAS u32x4*)(sm + VT + w * 2048 + l * 16) = pack8(av, 0);
    *(LAS u32x4*)(sm + VT + w * 2048 + 1024 + l * 16) = pack8(av, 8);
    STAMP(8);
    WAITV(0);
    BAR();
    STAMP(9);
    issueH(XR + 32768, 0, 1);
    lptr ta = sm + PT + w * 1024 + (16 * hh + 4 * blk + q4) * 32 + pp * 8;
    lptr vb = sm + VT + hh * 1024 + (16 * (w >> 1) + i16 + 32 * blk) * 16;
    const u32 one = 0x3F803F80u;
    f32x4 kv = {}, kz = {};
#pragma unroll
    for (int s4 = 0; s4 < 4; ++s4) {
      const u32x4 a = trd2(ta + s4 * 4096, ta + s4 * 4096 + 256);
      kv = mfma16(a, lds128(vb + s4 * 2048), kv);
      kz = mfma16(a, (u32x4){one, one, one, one}, kz);
    }
    const u32 pbase = WS_KVP + (j * 16 + p) * 2176;
    st64c(rs, pbase + w * 512 + i16 * 32 + g4 * 8, (u32x2){pk2(kv[0], kv[1]), pk2(kv[2], kv[3])});
    if (i16 == 0) st64c(rs, pbase + 2048 + w * 32 + g4 * 8, (u32x2){pk2(kz[0], kz[1]), pk2(kz[2], kz[3])});
    MEMB(); SCHEDB();
  }
  STAMP(10);
  const u32x4 so = mk_srd(wo, 512 * 1024);
  const u32 ov0 = ((l >> 2) & 7) * 1024 + ct * 128 + hh * 64 + pp * 16;
  constexpr int WOB[4] = {0, 16384, QA, 32768};
  auto issueO = [&](int i) NOPK { dma4(lb + WOB[i] + w * 4096, ov0, ov0 + 7168, ov0 + 2 * 7168, ov0 + 3 * 7168, so, (128 * w + 32 * i) * 1024); };
  f32x16 aq = {};
  {
    u32x4 wq[2][4];
    auto ldq = [&](int c) NOPK {
      lptr wsl = sm + ((c < 4) ? QA : XR + 32768) + (c & 3) * 4096 + wlane;
#pragma unroll
      for (int ks = 0; ks < 4; ++ks) wq[c & 1][ks] = trd2(wsl + ks * 1024, wsl + ks * 1024 + 256);
    };
    ldq(0);
#pragma unroll
    for (int c = 0; c < 8; ++c) {
      if (c < 7 && c != 3) ldq(c + 1);
      if (c >= 4 && c < 7) issueO(c - 4);
      SCHEDB();
#pragma unroll
      for (int ks = 0; ks < 4; ++ks) aq = mfma32(wq[c & 1][ks], xf[c * 4 + ks], aq);
      SCHEDB();
      if (c == 3) {
        WAITV(0);
        if (l == 0) set_flag(ws, WS_KVF + ((j * 4 + w) * 16 + p) * 128, tag);
        STAMP(11);
        BAR();
        ldq(4);
      }
      if (c == 6) BAR();
    }
  }
  STAMP(12);
  u32x4 pq[2][2];
  {
    const f32x16 zero = {};
    issueO(3);
    const f32x16 t0 = mfma32(phif[0], pack8(aq, 0), zero), t1 = mfma32(phif[1], pack8(aq, 8), zero);
    softmax16(t0, pq[0][0], pq[0][1]); PIN(pq[0][0]); PIN(pq[0][1]);
    softmax16(t1, pq[1][0], pq[1][1]); PIN(pq[1][0]); PIN(pq[1][1]);
  }
  u32x4 sel[2];
  {
    const u32 onev = (l & 1) ? 0x3F800000u : 0x00003F80u;
#pragma unroll
    for (int cg = 0; cg < 2; ++cg)
#pragma unroll
      for (int d = 0; d < 4; ++d) sel[cg][d] = ((i16 >> 1) == 4 * cg + d) ? onev : 0u;
  }
  STAMP(13);
  PIN(sel[0]); PIN(sel[1]);
  wait_flags(ws, WS_KVF + ((j * 4 + w) * 16 + i16) * 128, tag);
  STAMP(14);
  {
    const u32 base = WS_KVP + j * 16 * 2176;
    u32x4 v[8];
#pragma unroll
    for (int i = 0; i < 8; ++i) v[i] = ld128c(rs, base + (4 * (i >> 1) + g4) * 2176 + w * 512 + (i & 1) * 256 + i16 * 16);
    const u32x2 zv = ld64c(rs, base + (l >> 2) * 2176 + 2048 + w * 32 + pp * 8);
    MEMB(); SCHEDB();
    f32x4 a = {};
#pragma unroll
    for (int i = 0; i < 8; ++i) a = mfma16(sel[i & 1], v[i], a);
    *(LAS u32x2*)(sm + KVI + w * 1024 + (pp >> 1) * 512 + (16 + (l >> 2)) * 16 + (pp & 1) * 8) = zv;
    *(LAS u32x2*)(sm + KVI + w * 1024 + (l & 1) * 512 + (8 * hh + (i16 >> 1)) * 16 + blk * 8) = (u32x2){pk2(a[0], a[1]), pk2(a[2], a[3])};
  }
  WAITV(0);
  BAR();
  STAMP(15);
  u32x4 wf[32];
  {
    const f32x16 zero = {};
    lptr ap = sm + KVI + hh * 1024 + r32 * 16;
    f32x16 t[2];
#pragma unroll
    for (int h = 0; h < 2; ++h) { t[h] = mfma32(lds128(ap + h * 2048), pq[h][0], zero); t[h] = mfma32(lds128(ap + h * 2048 + 512), pq[h][1], t[h]); }
#pragma unroll
    for (int h = 0; h < 2; ++h) {
      const f32x2 d2 = ((f32x2){t[h][8], t[h][9]} + (f32x2){t[h][10], t[h][11]}) + ((f32x2){t[h][12], t[h][13]} + (f32x2){t[h][14], t[h][15]});
      const float dh = d2.x + d2.y;
      const auto sw2 = __builtin_amdgcn_permlane32_swap(f2u(dh), f2u(dh), false, false);
      const float r = __builtin_amdgcn_rcpf((u2f(sw2[0]) + u2f(sw2[1])) + 1e-6f);
      u32 o[4];
#pragma unroll
      for (int i = 0; i < 4; ++i) { const f32x2 q = (f32x2){t[h][2 * i], t[h][2 * i + 1]} * (f32x2){r, r}; o[i] = pk2(q.x, q.y); }
      st128c(rs, WS_OFR + ((4 * p + w) * 32 + 2 * j + h) * 1024 + l * 16, (u32x4){o[0], o[1], o[2], o[3]});
    }
    MEMB(); SCHEDB();
    STAMP(16);
    lptr wb = sm + kh * 8192 + (4 * hh + q4) * 64 + blk * 32 + pp * 8;
#pragma unroll
    for (int i = 0; i < 16; ++i) {
      lptr q = wb + WOB[(i >> 1) & 3] + (i >> 3) * 4096 + (i & 1) * 2048;
      wf[2 * i] = trd2(q, q + 1024); wf[2 * i + 1] = trd2(q + 512, q + 1536);
    }
    WAITV(0);
    if (l == 0) set_flag(ws, WS_OF + ((4 * p + w) * 16 + j) * 128, tag);
  }
  STAMP(17);
  BAR();
  __builtin_amdgcn_s_sleep(8);
  wait_flags(ws, WS_OF + (st * 16 + 8 * kh + (l & 7)) * 128, tag);
  STAMP(18);
  f32x16 acc0 = {}, acc1 = {};
  {
    const u32 obase = WS_OFR + (st * 32 + 16 * kh) * 1024 + l * 16;
    u32x4 of[16];
#pragma unroll
    for (int i = 0; i < 16; ++i) of[i] = ld128c(rs, obase + i * 1024);
    MEMB(); SCHEDB();
#pragma unroll
    for (int i = 0; i < 16; ++i) { acc0 = mfma32(wf[2 * i], of[i], acc0); acc1 = mfma32(wf[2 * i + 1], of[i], acc1); }
  }
  STAMP(19);
  {
    lptr rp = sm + kh * 17408 + (32 * sh + r32) * 272 + hh * 16;
#pragma unroll
    for (int g = 0; g < 4; ++g) *(LAS f32x4*)(rp + g * 32) = (f32x4){acc0[4 * g], acc0[4 * g + 1], acc0[4 * g + 2], acc0[4 * g + 3]};
#pragma unroll
    for (int g = 0; g < 4; ++g) *(LAS f32x4*)(rp + 128 + g * 32) = (f32x4){acc1[4 * g], acc1[4 * g + 1], acc1[4 * g + 2], acc1[4 * g + 3]};
    BAR();
#pragma unroll
    for (int i = 0; i < 2; ++i) {
      const int row = i * 32 + (tid >> 3), oc = tid & 7;
      lptr sp = sm + row * 272 + oc * 32;
      const f32x4 a = *(LAS f32x4*)sp + *(LAS f32x4*)(sp + 17408), bb = *(LAS f32x4*)(sp + 16) + *(LAS f32x4*)(sp + 17408 + 16);
      *(u32x4*)(out + (size_t)(64 * rt + row) * 2048 + ct * 128 + oc * 16) = (u32x4){pk2(a[0], a[1]), pk2(a[2], a[3]), pk2(bb[0], bb[1]), pk2(bb[2], bb[3])};
    }
  }
  if (b == 0 && tid == 0) set_flag(ws, WS_EPOCH, epoch + 1);
  STAMP(20);
  STAMPS_OUT();
}

DI f32x2 cmul(f32x2 a, f32x2 b) { return (f32x2){__builtin_fmaf(-a.y, b.y, a.x * b.x), __builtin_fmaf(a.y, b.x, a.x * b.y)}; }
DI f32x2 cmulc(f32x2 a, f32x2 b) { return (f32x2){__builtin_fmaf(a.y, b.y, a.x * b.x), __builtin_fmaf(-a.x, b.y, a.y * b.x)}; }
DI f32x2 cexp_s(float a2, float brev, float s) {
  const float m = __builtin_amdgcn_exp2f(a2 * s);
  const float th = __builtin_amdgcn_fractf(brev * s);
  return (f32x2){m * __builtin_amdgcn_cosf(th), m * __builtin_amdgcn_sinf(th)};
}
template <int OFF> DI void wr_tid(u32 v) { asm volatile("ds_write_addtid_b32 %0 offset:%1" :: "v"(v), "i"(OFF) : "memory"); }

template <int T0, int T1, int T2, int T3>
DI void conv_tiles(lptr ka, lptr ub, int ust, f32x4 (&acc)[4][4]) {
  constexpr int T[4] = {T0, T1, T2, T3};
  constexpr int NB = 2 * T3 + 2, PF = 4;
  u32x4 af[NB], bfr[4 * NB];
  bool have[NB] = {};
  auto lda = [&](int m) NOPK { af[m] = trd2(ka + m * 256, ka + m * 256 - 32); };
  auto ldb = [&](int n) NOPK {
    lptr q = ub + (n / NB) * ust + (n % NB) * 64;
    const u32x2 x0 = lds64(q), x1 = lds64(q + 8);
    bfr[n] = (u32x4){x0.x, x0.y, x1.x, x1.y};
  };
  auto need = [&](int n) NOPK {
    ldb(n);
    if (n < NB) {
#pragma unroll
      for (int t = 0; t < 4; ++t) { const int m = 2 * T[t] + 1 - n; if (m >= 0 && !have[m]) { lda(m); have[m] = true; } }
    }
  };
  need(0); need(1);
#pragma unroll
  for (int n = 0; n < 4 * NB; ++n) {
    if (n < 2) { need(2 * n + 2); need(2 * n + 3); }
    else if (n + PF < 4 * NB) need(n + PF);
    SCHEDB();
    const int rho = n / NB, be = n % NB;
#pragma unroll
    for (int t = 0; t < 4; ++t) {
      const int m = 2 * T[t] + 1 - be;
      if (m >= 0) acc[t][rho] = mfma16(af[m], bfr[n], be == 0 ? (f32x4){} : acc[t][rho]);
    }
    SCHEDB();
  }
}

DI void role_B(lptr sm, const int b, const char* u, const u16* Are, const u16* Aim, const u16* Bre, const u16* Bim, const u16* Cre, const u16* Cim,
               const u16* ldt, const char* Dsk, char* out, char* ws) {
  const int gam = b & 7, idx = b >> 3;
  const int tid = threadIdx.x, l = tid & 63, w = __builtin_amdgcn_readfirstlane(tid >> 6);
  const int g4 = l >> 4, i16 = l & 15, q4 = i16 >> 2, pp = l & 3;
  constexpr int TROW = 528, TCH = 57 * TROW;
  constexpr int UT = 60416;
  constexpr int UP = 60416, UST = 1152;
  constexpr int KL = 69632, KST = 4448;
  constexpr int CB = 0, CST = 8208;
  constexpr int YS = 16640;
  const RSRC rs = __builtin_amdgcn_make_buffer_rsrc(ws, 0, WS_END, 0x00020000);
  STAMPS_DECL; STAMP(0);
  u32x4 ur[2];
  const char* urow = u + (size_t)(64 * idx + 16 * w + i16) * 1024 + gam * 128 + g4 * 16;
  ur[0] = *(const u32x4*)urow; ur[1] = *(const u32x4*)(urow + 64);
  const int ch = 64 * gam + 2 * idx + (w >> 1), pi = (64 * gam + 2 * idx) * 128 + tid;
  const u16 r_are = Are[pi], r_aim = Aim[pi], r_bre = Bre[pi], r_bim = Bim[pi], r_cre = Cre[pi], r_cim = Cim[pi], r_ld = ldt[ch];
  const u64 epoch = sload64(ws + WS_EPOCH + 128 * (1 + gam));
  const u64 tag = mk_tag(epoch);
  *(LAS u32x4*)(sm + UT + (16 * w + i16) * 144 + g4 * 16) = ur[0];
  *(LAS u32x4*)(sm + UT + (16 * w + i16) * 144 + 64 + g4 * 16) = ur[1];
  BAR();
#pragma unroll
  for (int e = 0; e < 2; ++e) {
    const int rho = 2 * e + (g4 >> 1), i0 = 8 * (g4 & 1);
    lptr a = sm + UT + (4 * (i0 + q4) + rho) * 144 + w * 32 + pp * 8;
    st128c(rs, WS_UX + (64 * gam + 16 * w + i16) * 4096 + idx * 128 + (4 * e + g4) * 16, trd2(a, a + 16 * 144));
  }
  MEMB(); SCHEDB();
  STAMP(1);
  {
    const float dt = rbf(__builtin_amdgcn_exp2f(bf1(r_ld) * 1.4426950408889634f));
    const float a2 = (dt * bf1(r_are)) * 1.4426950408889634f, brev = (dt * bf1(r_aim)) * 0.15915494309189535f;
    { float t_ = a2 + brev; PIN(t_); } STAMP(2);
    asm volatile("s_mov_b32 m0, %0\n\ts_nop 0" :: "s"((u32)(size_t)sm + (w >> 1) * TCH + (w & 1) * 256) : "memory");
#define PUT(row, v) wr_tid<(row) * TROW>(pk2((v).x, (v).y))
    const f32x2 w8 = cexp_s(a2, brev, 8.f), w1 = cexp_s(a2, brev, 1.f), w128 = cexp_s(a2, brev, 128.f);
    f32x2 e[16], h[17];
    wr_tid<0>(0x00003F80u);
    h[0] = cmulc((f32x2){bf1(r_cre), -bf1(r_cim)}, (f32x2){dt * bf1(r_bre), dt * bf1(r_bim)});
    e[1] = w8; e[2] = cmul(w8, w8);
    const f32x2 w256 = cmul(w128, w128), w2 = cmul(w1, w1);
    h[1] = cmulc(h[0], w128);
    PUT(1, e[1]); PUT(16, h[0]);
    e[3] = cmul(e[2], w8); e[4] = cmul(e[2], e[2]);
    const f32x2 w512 = cmul(w256, w256), w4 = cmul(w2, w2);
    h[2] = cmulc(h[0], w256); h[3] = cmulc(h[1], w256);
    PUT(2, e[2]); PUT(24, h[1]); PUT(3, e[3]); PUT(4, e[4]);
    e[5] = cmul(e[4], e[1]); e[6] = cmul(e[4], e[2]); e[7] = cmul(e[4], e[3]); e[8] = cmul(e[4], e[4]);
    PUT(32, h[2]); PUT(36, h[3]);
    h[4] = cmulc(h[0], w512); h[5] = cmulc(h[1], w512); h[6] = cmulc(h[2], w512); h[7] = cmulc(h[3], w512);
    PUT(5, e[5]); PUT(6, e[6]); PUT(7, e[7]); PUT(8, e[8]);
    e[9] = cmul(e[8], e[1]); e[10] = cmul(e[8], e[2]); e[11] = cmul(e[8], e[3]); e[12] = cmul(e[8], e[4]);
    PUT(40, h[4]); PUT(42, h[5]); PUT(44, h[6]); PUT(46, h[7]);
    h[8] = cmulc(h[4], w512); h[9] = cmulc(h[5], w512); h[10] = cmulc(h[6], w512); h[11] = cmulc(h[7], w512);
    PUT(9, e[9]); PUT(10, e[10]); PUT(11, e[11]); PUT(12, e[12]);
    e[13] = cmul(e[8], e[5]); e[14] = cmul(e[8], e[6]); e[15] = cmul(e[8], e[7]);
    PUT(48, h[8]); PUT(49, h[9]); PUT(50, h[10]); PUT(51, h[11]);
    h[12] = cmulc(h[8], w512); h[13] = cmulc(h[9], w512); h[14] = cmulc(h[10], w512); h[15] = cmulc(h[11], w512);
    PUT(13, e[13]); PUT(14, e[14]); PUT(15, e[15]);
    { const f32x2 a = cmulc(h[4], w4), bq = cmulc(h[5], w4), c = cmulc(h[6], w4), d = cmulc(h[7], w4);
      PUT(52, h[12]); PUT(53, h[13]); PUT(54, h[14]); PUT(55, h[15]);
      h[16] = cmulc(h[12], w512);
      const f32x2 a1 = cmulc(h[0], w1), a2_ = cmulc(h[0], w2), b1 = cmulc(h[1], w1), b2 = cmulc(h[1], w2);
      PUT(41, a); PUT(43, bq); PUT(45, c); PUT(47, d); PUT(56, h[16]);
      const f32x2 a3 = cmulc(a1, w2), b3 = cmulc(b1, w2), a4 = cmulc(h[0], w4), b4 = cmulc(h[1], w4);
      PUT(17, a1); PUT(18, a2_); PUT(25, b1); PUT(26, b2);
      const f32x2 a5 = cmulc(a1, w4), a6 = cmulc(a2_, w4), b5 = cmulc(b1, w4), b6 = cmulc(b2, w4);
      PUT(19, a3); PUT(27, b3); PUT(20, a4); PUT(28, b4);
      const f32x2 a7 = cmulc(a3, w4), b7 = cmulc(b3, w4), c1 = cmulc(h[2], w2), d1 = cmulc(h[3], w2);
      PUT(21, a5); PUT(22, a6); PUT(29, b5); PUT(30, b6);
      const f32x2 c2 = cmulc(h[2], w4), d2 = cmulc(h[3], w4), c3 = cmulc(c1, w4), d3 = cmulc(d1, w4);
      PUT(23, a7); PUT(31, b7); PUT(33, c1); PUT(37, d1);
      PUT(34, c2); PUT(38, d2); PUT(35, c3); PUT(39, d3);
    }
#undef PUT
  }
  STAMP(3);
  WAITV(0);
  STAMP(4);
  if (l == 0) set_flag(ws, uf_off(b, w), tag);
  BAR();
  STAMP(5);
  {
    lptr tb = sm + (w >> 1) * TCH + g4 * 16;
    lptr kb = sm + KL + (w >> 1) * KST + 32;
    if ((w & 1) == 0) {
      f32x4 d0 = {}, d1 = {};
#pragma unroll
      for (int ks = 0; ks < 8; ++ks) {
        const u32x4 e = lds128(tb + i16 * TROW + ks * 64);
        d0 = mfma16(lds128(tb + (16 + i16) * TROW + ks * 64), e, d0);
        d1 = mfma16(lds128(tb + (32 + i16) * TROW + ks * 64), e, d1);
      }
      const u32 p00 = pk2(d1[0], d1[0]), p22 = pk2(d1[2], d1[2]);
      if (g4 >= 2) {
        lptr a = kb + (256 * g4 + 8 * i16) * 2;
        *(LAS u32*)(a - 4) = p00;       *(LAS u32x2*)a = (u32x2){p00, pk2(d1[0], d1[1])};         *(LAS u32*)(a + 8) = pk2(d1[1], d1[1]);
        *(LAS u32*)(a + 252) = p22;     *(LAS u32x2*)(a + 256) = (u32x2){p22, pk2(d1[2], d1[3])}; *(LAS u32*)(a + 264) = pk2(d1[3], d1[3]);
      }
      MEMB();
      if (g4 < 2) {
        lptr a = kb + (256 + 128 * g4 + 8 * i16) * 2;
        *(LAS u32x2*)a = (u32x2){p00, pk2(d1[1], d1[2])}; *(LAS u32*)(a + 8) = p22;
        *(LAS u16*)(a + 12) = (u16)pk2(d1[3], d1[3]);     *(LAS u16*)(a - 2) = (u16)p00;
      }
      MEMB();
      *(LAS u32x2*)(kb + (128 * (g4 >> 1) + 8 * i16 + 4 * (g4 & 1)) * 2) = (u32x2){pk2(d0[0], d0[1]), pk2(d0[2], d0[3])};
    } else {
      const int c8 = i16 < 8 ? i16 : 8;
      f32x4 d2 = {};
#pragma unroll
      for (int ks = 0; ks < 8; ++ks) d2 = mfma16(lds128(tb + i16 * TROW + ks * 64), lds128(tb + (48 + c8) * TROW + ks * 64), d2);
      const u32 p00 = pk2(d2[0], d2[0]), p11 = pk2(d2[1], d2[1]), p22 = pk2(d2[2], d2[2]), p33 = pk2(d2[3], d2[3]);
      lptr a = kb + (1024 + 128 * c8 + 32 * g4) * 2;
      *(LAS u32*)(l == 0 ? kb + 2200 * 2 : a - 8) = p00;
      *(LAS u32*)(a - 4) = p00;
      *(LAS u32x4*)a = (u32x4){p00, p00, pk2(d2[0], d2[1]), p11};
      *(LAS u32x4*)(a + 16) = (u32x4){p11, p11, p22, p22};
      *(LAS u32x4*)(a + 32) = (u32x4){p22, p22, pk2(d2[2], d2[3]), p33};
      *(LAS u32x2*)(a + 48) = (u32x2){p33, p33};
      if (l < 34) *(LAS u32x4*)(l < 32 ? sm + UP + ((w >> 1) * 4 + (l >> 3)) * UST + (l & 7) * 16 : kb - 32 + (l - 32) * 16) = (u32x4){0, 0, 0, 0};
    }
  }
  STAMP(6);
  wait_flags(ws, uf_off(gam + 8 * (8 * w + (l & 7)), idx >> 3), tag);
  STAMP(7);
  {
    u32x4 v[2];
#pragma unroll
    for (int e = 0; e < 2; ++e) v[e] = ld128c(rs, WS_UX + (64 * gam + 2 * idx + e) * 4096 + tid * 16);
#pragma unroll
    for (int e = 0; e < 2; ++e) *(LAS u32x4*)(sm + UP + (e * 4 + ((tid >> 1) & 3)) * UST + 128 + (tid >> 3) * 32 + (tid & 1) * 16) = v[e];
  }
  u32x4 dv[2];
  dv[0] = *(const u32x4*)(Dsk + gam * 128 + g4 * 16); dv[1] = *(const u32x4*)(Dsk + gam * 128 + 64 + g4 * 16);
  BAR();
  if (idx == 0 && tid == 0) set_flag(ws, WS_EPOCH + 128 * (1 + gam), epoch + 1);
  STAMP(8);
  {
    const int c = w >> 1, hs = w & 1;
    f32x4 acc[4][4];
    lptr ka = sm + KL + c * KST + 32 + (4 * pp - 4 * (8 * g4 + q4) + 112) * 2;
    lptr ub = sm + UP + c * 4 * UST + 128 + (4 * i16 + 8 * g4 - 60) * 2;
    if (hs == 0) conv_tiles<0, 3, 4, 7>(ka, ub, UST, acc); else conv_tiles<1, 2, 5, 6>(ka, ub, UST, acc);
    STAMP(9);
    lptr cb = sm + CB + c * CST + 16;
    f32x4 y[4];
#pragma unroll
    for (int t = 0; t < 4; ++t) {
      const int tau = 2 * t + (((t & 1) ^ hs) ? 1 : 0);
      const f32x4 *z = acc[t];
      *(LAS f32x4*)(cb + (64 * tau + l) * 16) = (f32x4){(z[1][3] + z[2][2]) + z[3][1], z[2][3] + z[3][2], z[3][3], 0.f};
      y[t] = (f32x4){z[0][0], z[0][1] + z[1][0], (z[0][2] + z[1][1]) + z[2][0], (z[0][3] + z[1][2]) + (z[2][1] + z[3][0])};
    }
    if (hs == 0 && l == 0) *(LAS f32x4*)(cb - 16) = (f32x4){};
    BAR();
    STAMP(10);
    const int src = g4 > 0 ? l - 16 : (i16 > 0 ? 47 + i16 : -1);
    u32x2 yp[4];
#pragma unroll
    for (int t = 0; t < 4; ++t) {
      const int tau = 2 * t + (((t & 1) ^ hs) ? 1 : 0);
      const f32x4 r = y[t] + *(LAS f32x4*)(cb + (64 * tau + src) * 16);
      yp[t] = (u32x2){pk2(r[0], r[1]), pk2(r[2], r[3])};
    }
#pragma unroll
    for (int t = 0; t < 4; t += 2) {
      const auto s0 = __builtin_amdgcn_permlane16_swap(yp[t].x, yp[t + 1].x, false, false);
      const auto s1 = __builtin_amdgcn_permlane16_swap(yp[t].y, yp[t + 1].y, false, false);
      const int tau = (g4 & 1) ? 2 * t + 3 - hs : 2 * t + hs;
      st128c(rs, WS_YT + (64 * gam + 2 * idx + c) * 4096 + tau * 512 + i16 * 32 + (g4 >> 1) * 16, (u32x4){s0[0], s1[0], s0[1], s1[1]});
    }
  }
  MEMB(); SCHEDB();
  u32x4 sk[2];
#pragma unroll
  for (int e = 0; e < 2; ++e) {
#pragma unroll
    for (int d = 0; d < 4; ++d) sk[e][d] = pk2(bf_lo(ur[e][d]) * bf_lo(dv[e][d]), bf_hi(ur[e][d]) * bf_hi(dv[e][d]));
    PIN(sk[e]);
  }
  STAMP(11);
  WAITV(0);
  if (l == 0) set_flag(ws, WS_YF + ((gam * 4 + w) * 32 + idx) * 128, tag);
  STAMP(12);
  __builtin_amdgcn_s_sleep(8);
  {
    const int tl = idx >> 2, hsn = ((tl >> 1) ^ tl) & 1;
    wait_flags(ws, WS_YF + ((gam * 4 + 2 * ((l >> 3) & 1) + hsn) * 32 + 8 * w + (l & 7)) * 128, tag);
  }
  STAMP(13);
  {
    u32x4 v[2];
#pragma unroll
    for (int e = 0; e < 2; ++e) v[e] = ld128c(rs, WS_YT + (64 * gam + 16 * w + 8 * e + (l >> 3)) * 4096 + idx * 128 + (l & 7) * 16);
#pragma unroll
    for (int e = 0; e < 2; ++e) *(LAS u32x4*)(sm + YS + (16 * w + 8 * e + (l >> 3)) * 144 + (l & 7) * 16) = v[e];
  }
  BAR();
#pragma unroll
  for (int e = 0; e < 2; ++e) {
    lptr a = sm + YS + (8 * (4 * e + g4) + q4) * 144 + w * 32 + pp * 8;
    const u32x4 y = trd2(a, a + 4 * 144);
    u32x4 o;
#pragma unroll
    for (int d = 0; d < 4; ++d) o[d] = pk2(bf_lo(y[d]) + bf_lo(sk[e][d]), bf_hi(y[d]) + bf_hi(sk[e][d]));
    *(u32x4*)(out + (size_t)(64 * idx + 16 * w + i16) * 2048 + 1024 + gam * 128 + e * 64 + g4 * 16) = o;
  }
  STAMP(14);
  STAMPS_OUT();
}

#define RUN_A 1
#define RUN_B 1
__global__ __launch_bounds__(256, 2) NOPK void fused_kernel(const char* x, const char* W, const char* wphi, const char* wo, const char* u,
                                                       const u16* Are, const u16* Aim, const u16* Bre, const u16* Bim, const u16* Cre, const u16* Cim,
                                                       const u16* ldt, const char* Dsk, char* out, char* ws) {
  extern __shared__ __attribute__((aligned(16))) char smem[];
  lptr sm = (lptr)smem;
  if (blockIdx.x < 256) { if (RUN_A) role_A(sm, blockIdx.x, x, W, wphi, wo, out, ws); }
  else { if (RUN_B) role_B(sm, blockIdx.x - 256, u, Are, Aim, Bre, Bim, Cre, Cim, ldt, Dsk, out, ws); }
}

extern "C" void kernel_launch(void* const* d_in, const int* in_sizes, int n_in, void* d_out, int out_size, void* d_ws, size_t ws_size,
                              hipStream_t stream) {
  (void)hipFuncSetAttribute((const void*)fused_kernel, hipFuncAttributeMaxDynamicSharedMemorySize, LDS_BYTES);
  fused_kernel<<<512, 256, LDS_BYTES, stream>>>((const char*)d_in[0], (const char*)d_in[1], (const char*)d_in[2], (const char*)d_in[3], (const char*)d_in[4],
                                                (const u16*)d_in[5], (const u16*)d_in[6], (const u16*)d_in[7], (const u16*)d_in[8], (const u16*)d_in[9], (const u16*)d_in[10],
                                                (const u16*)d_in[11], (const char*)d_in[12], (char*)d_out, (char*)d_ws);
}
```

```cpp
#include <hip/hip_runtime.h>
#include <hip/hip_bf16.h>

typedef unsigned int u32;
typedef unsigned long long u64;
typedef unsigned short u16;
typedef short s16x4 __attribute__((ext_vector_type(4)));
typedef short s16x8 __attribute__((ext_vector_type(8)));
typedef __bf16 bf16x2 __attribute__((ext_vector_type(2)));
typedef __bf16 bf16x8 __attribute__((ext_vector_type(8)));
typedef float f32x2 __attribute__((ext_vector_type(2)));
typedef float f32x4 __attribute__((ext_vector_type(4)));
typedef float f32x16 __attribute__((ext_vector_type(16)));
typedef u32 u32x2 __attribute__((ext_vector_type(2)));
typedef u32 u32x4 __attribute__((ext_vector_type(4)));
#define LAS __attribute__((address_space(3)))
#define GAS __attribute__((address_space(1)))
#if defined(__HIP_DEVICE_COMPILE__)
#define NOPK __attribute__((target("no-packed-fp32-ops")))
#else
#define NOPK
#endif
#define DI __device__ __forceinline__ NOPK
typedef LAS char* lptr;

#define PSLEEP 16
#define OPRE 20
#define YPRE 20
#define UFK 1
#define APRIO 2
#define BP0 0
#define BP1 0
#define BP2 0
#define ADELAY 2
#define CONVT 0
#define KVFC 4
#define KSPLIT 3
#define OAUX 17
#define SWAP_ROLES 1
constexpr u32 WS_EPOCH = 0;
constexpr u32 WS_OF = 4096;
constexpr u32 WS_UF = WS_OF + 1024 * 128;
constexpr u32 WS_YF = WS_UF + 1024 * 128;
constexpr u32 WS_KVF = WS_YF + 1024 * 128;
constexpr u32 WS_KVP = 1u << 20;
constexpr u32 KVS = 2176;
constexpr u32 WS_OFR = 2u << 20;
constexpr u32 WS_UX = 4u << 20;
constexpr u32 WS_YT = 6u << 20;
constexpr u32 WS_END = 8u << 20;
constexpr u32 OOB = 0x7FFFFFF0u;
constexpr int LDS_BYTES = 81920;
constexpr u32 SPIN_CAP = 1u << 16;

#define STAMPS_DECL
#define STAMP(k)
#define STAMPS_OUT()

#define BAR() asm volatile("s_waitcnt lgkmcnt(0)\n\ts_barrier" ::: "memory")
#define WAITV(n) asm volatile("s_waitcnt vmcnt(" #n ")" ::: "memory")
#define MEMB() asm volatile("" ::: "memory")
#define SCHEDB() __builtin_amdgcn_sched_barrier(0)

DI float u2f(u32 v) { return __builtin_bit_cast(float, v); }
DI u32 f2u(float v) { return __builtin_bit_cast(u32, v); }
DI bool all_lanes(bool c) { return __builtin_amdgcn_ballot_w64(!c) == 0; }
DI u32 pk2(float a, float b) { f32x2 v = {a, b}; return __builtin_bit_cast(u32, __builtin_convertvector(v, bf16x2)); }
DI float bf_lo(u32 v) { return u2f(v << 16); }
DI float bf_hi(u32 v) { return u2f(v & 0xffff0000u); }
DI float bf1(u16 v) { return u2f((u32)v << 16); }
DI float rbf(float a) { return bf_lo(pk2(a, a)); }
DI u32x2 trd(lptr p) { return __builtin_bit_cast(u32x2, __builtin_amdgcn_ds_read_tr16_b64_v4i16((LAS s16x4*)p)); }
DI u32x4 trd2(lptr p0, lptr p1) { u32x2 a = trd(p0), b = trd(p1); return (u32x4){a.x, a.y, b.x, b.y}; }
DI u32x4 lds128(lptr p) { return *(LAS u32x4*)p; }
DI u32x2 lds64(lptr p) { return *(volatile LAS u32x2*)p; }
DI f32x16 mfma32(u32x4 a, u32x4 b, f32x16 c) { return __builtin_amdgcn_mfma_f32_32x32x16_bf16(__builtin_bit_cast(bf16x8, a), __builtin_bit_cast(bf16x8, b), c, 0, 0, 0); }
DI f32x4 mfma16(u32x4 a, u32x4 b, f32x4 c) { return __builtin_amdgcn_mfma_f32_16x16x32_bf16(__builtin_bit_cast(bf16x8, a), __builtin_bit_cast(bf16x8, b), c, 0, 0, 0); }
DI u32x4 mk_srd(const void* p, u32 bytes) { const u64 a = (u64)p; return (u32x4){(u32)a, (u32)(a >> 32) & 0xffffu, bytes, 0x00020000u}; }
DI void dma1(u32 lds, u32 v0, u32x4 srd, u32 soff) {
  asm volatile("s_mov_b32 m0, %0\n\ts_nop 0\n\tbuffer_load_dwordx4 %1, %2, %3 offen lds" :: "s"(lds), "v"(v0), "s"(srd), "s"(soff) : "memory");
}
DI void dma2(u32 lds, u32 v0, u32x4 srd, u32 soff) {
  asm volatile("s_mov_b32 m0, %0\n\ts_nop 0\n\tbuffer_load_dwordx4 %1, %2, %3 offen lds\n\tbuffer_load_dwordx4 %1, %2, %3 offen offset:1024 lds"
               :: "s"(lds), "v"(v0), "s"(srd), "s"(soff) : "memory");
}
DI void dma4(u32 lds, u32 v0, u32 v1, u32 v2, u32 v3, u32x4 srd, u32 soff) {
  asm volatile("s_mov_b32 m0, %0\n\ts_nop 0\n\tbuffer_load_dwordx4 %1, %5, %6 offen lds\n\tbuffer_load_dwordx4 %2, %5, %6 offen offset:1024 lds\n\t"
               "buffer_load_dwordx4 %3, %5, %6 offen offset:2048 lds\n\tbuffer_load_dwordx4 %4, %5, %6 offen offset:3072 lds"
               :: "s"(lds), "v"(v0), "v"(v1), "v"(v2), "v"(v3), "s"(srd), "s"(soff) : "memory");
}
DI void setm0(u32 lds) { asm volatile("s_mov_b32 m0, %0\n\ts_nop 0" :: "s"(lds) : "memory"); }
template <int OFF> DI void dmao(u32 v, u32x4 srd, u32 soff) {
  asm volatile("buffer_load_dwordx4 %0, %1, %2 offen offset:%3 lds" :: "v"(v), "s"(srd), "s"(soff), "i"(OFF) : "memory");
}
#define PIN(x) asm volatile("" : "+v"(x))
DI u32 mk_tag(u32 e) { const u32 t = (e + 0x1B4E0193u) * 0x2F4A7C15u; return (t & 0x3FFFu) | ((t & 0x1FFFC000u) << 1) | ((t & 0x20000000u) << 2); }
DI u32 mk_tag2(u32 e) { return (e ^ 0x5BD1E995u) * 0x9E3779B1u; }
#define RSRC __amdgpu_buffer_rsrc_t
DI void st128c(RSRC r, u32 off, u32x4 v) { __builtin_amdgcn_raw_buffer_store_b128(v, r, off, 0, 16); }
DI void st64c(RSRC r, u32 off, u32x2 v) { __builtin_amdgcn_raw_buffer_store_b64(v, r, off, 0, 16); }
DI void st32c(RSRC r, u32 off, u32 v) { __builtin_amdgcn_raw_buffer_store_b32(v, r, off, 0, 16); }
DI u32x4 ld128c(RSRC r, u32 off) { return __builtin_amdgcn_raw_buffer_load_b128(r, off, 0, 16); }
DI u32x2 ld64c(RSRC r, u32 off) { return __builtin_amdgcn_raw_buffer_load_b64(r, off, 0, 16); }
DI bool flag_ok(u32x2 f, u32x2 tg) { return all_lanes(f.x == tg.x && f.y == tg.y); }
DI void wait_flags(RSRC r, u32 off, u32x2 tg) {
#pragma unroll 1
  for (u32 spins = 0; spins < SPIN_CAP; ++spins) {
    MEMB();
    if (flag_ok(ld64c(r, off), tg)) break;
    __builtin_amdgcn_s_sleep(PSLEEP);
  }
  MEMB();
}
DI u32 tag_diff(u32 d, u32x4 v, u32 tag) { return d | (v.y ^ tag) | (v.w ^ tag); }
DI u32 uf_off(int blk, int w) { return WS_UF + (((blk & 7) * 4 + w) * 32 + (blk >> 3)) * 128; }

struct GB { u32 wr, rd; };
template <int TARGET> DI void gbar(const GB& g) {
  u32 t;
  asm volatile("v_mov_b32 %0, %3\n\ts_waitcnt lgkmcnt(0)\n\tds_write_b32 %1, %0\n"
               "1:\n\tds_read_b32 %0, %2\n\ts_waitcnt lgkmcnt(0)\n\tv_cmp_gt_u32 vcc, %3, %0\n\ts_cbranch_vccnz 1b"
               : "=&v"(t) : "v"(g.wr), "v"(g.rd), "i"(TARGET) : "memory", "vcc");
}

template <int TARGET> DI void gpost(const GB& g) {
  u32 t;
  asm volatile("v_mov_b32 %0, %2\n\tds_write_b32 %1, %0" : "=&v"(t) : "v"(g.wr), "i"(TARGET) : "memory");
}
template <int TARGET> DI void gcheck(const GB& g) {
  u32 t;
  asm volatile("1:\n\tds_read_b32 %0, %1\n\ts_waitcnt lgkmcnt(0)\n\tv_cmp_gt_u32 vcc, %2, %0\n\ts_cbranch_vccnz 1b"
               : "=&v"(t) : "v"(g.rd), "i"(TARGET) : "memory", "vcc");
}
template <int TARGET> DI void gverify(const GB& g, u32 pv) {
  asm volatile("v_cmp_gt_u32 vcc, %2, %0\n\ts_cbranch_vccz 2f\n"
               "1:\n\tds_read_b32 %0, %1\n\ts_waitcnt lgkmcnt(0)\n\tv_cmp_gt_u32 vcc, %2, %0\n\ts_cbranch_vccnz 1b\n2:"
               : "+v"(pv) : "v"(g.rd), "i"(TARGET) : "memory", "vcc");
}
template <int N> struct IC { static constexpr int v = N; };
template <int N> DI void waitv() { asm volatile("s_waitcnt vmcnt(%0)" :: "i"(N) : "memory"); }

DI float max3f(float a, float b, float c) { float r; asm("v_max3_f32 %0, %1, %2, %3" : "=v"(r) : "v"(a), "v"(b), "v"(c)); return r; }
template <class H> DI void softmax16h(const f32x16& p, u32x4& lo, u32x4& hi, H hook) {
  const float m0 = __builtin_fmaxf(p[0], p[1]);
  const float m1 = __builtin_fmaxf(p[14], p[15]);
  float ma = max3f(m0, p[2], p[3]), mb_ = max3f(m1, p[4], p[5]);
  ma = max3f(ma, p[6], p[7]); mb_ = max3f(mb_, p[8], p[9]);
  ma = max3f(ma, p[10], p[11]); mb_ = max3f(mb_, p[12], p[13]);
  const float L2E = 1.4426950408889634f;
  const float mb = max3f(ma, mb_, mb_) * -L2E;
  hook(IC<0>{});
  float e[16];
#pragma unroll
  for (int i = 0; i < 4; ++i) e[i] = __builtin_amdgcn_exp2f(__builtin_fmaf(p[i], L2E, mb));
  hook(IC<1>{});
#pragma unroll
  for (int i = 4; i < 8; ++i) e[i] = __builtin_amdgcn_exp2f(__builtin_fmaf(p[i], L2E, mb));
  hook(IC<2>{});
#pragma unroll
  for (int i = 8; i < 12; ++i) e[i] = __builtin_amdgcn_exp2f(__builtin_fmaf(p[i], L2E, mb));
  hook(IC<3>{});
#pragma unroll
  for (int i = 12; i < 16; ++i) e[i] = __builtin_amdgcn_exp2f(__builtin_fmaf(p[i], L2E, mb));
  hook(IC<4>{});
  const float s0 = ((e[0] + e[1]) + (e[2] + e[3])) + ((e[4] + e[5]) + (e[6] + e[7]));
  const float s1 = ((e[8] + e[9]) + (e[10] + e[11])) + ((e[12] + e[13]) + (e[14] + e[15]));
  const float r = __builtin_amdgcn_rcpf(s0 + s1);
  hook(IC<5>{});
  lo = (u32x4){pk2(e[0] * r, e[1] * r), pk2(e[2] * r, e[3] * r), pk2(e[4] * r, e[5] * r), pk2(e[6] * r, e[7] * r)};
  hook(IC<6>{});
  hi = (u32x4){pk2(e[8] * r, e[9] * r), pk2(e[10] * r, e[11] * r), pk2(e[12] * r, e[13] * r), pk2(e[14] * r, e[15] * r)};
  hook(IC<7>{});
}
DI void softmax16(const f32x16& p, u32x4& lo, u32x4& hi) { softmax16h(p, lo, hi, [](auto) NOPK {}); }
DI u32x4 pack8(const f32x16& a, int o) { return (u32x4){pk2(a[o], a[o + 1]), pk2(a[o + 2], a[o + 3]), pk2(a[o + 4], a[o + 5]), pk2(a[o + 6], a[o + 7])}; }

DI f32x2 cmul(f32x2 a, f32x2 b) { return (f32x2){__builtin_fmaf(-a.y, b.y, a.x * b.x), __builtin_fmaf(a.y, b.x, a.x * b.y)}; }
DI f32x2 cmulc(f32x2 a, f32x2 b) { return (f32x2){__builtin_fmaf(a.y, b.y, a.x * b.x), __builtin_fmaf(-a.x, b.y, a.y * b.x)}; }
DI f32x2 cexp_s(float a2, float brev, float s) {
  const float m = __builtin_amdgcn_exp2f(a2 * s);
  const float th = __builtin_amdgcn_fractf(brev * s);
  return (f32x2){m * __builtin_amdgcn_cosf(th), m * __builtin_amdgcn_sinf(th)};
}
template <int I> DI float mulb(float q, float x) {
  return __builtin_bit_cast(float, __builtin_amdgcn_update_dpp(0, __builtin_bit_cast(int, q), 0x150 + I, 0xf, 0xf, true)) * x;
}
template <int I> DI float fmab(float q, float x, float acc) {
  asm("v_fmac_f32_dpp %0, %1, %2 row_newbcast:%3 row_mask:0xf bank_mask:0xf" : "+v"(acc) : "v"(q), "v"(x), "i"(I));
  return acc;
}
template <int I> DI float fnmab(float q, float x, float acc) {
  asm("v_fmac_f32_dpp %0, -%1, %2 row_newbcast:%3 row_mask:0xf bank_mask:0xf" : "+v"(acc) : "v"(q), "v"(x), "i"(I));
  return acc;
}
template <int I> DI f32x2 cexp_b(float a2, float brev, float s) {
  const float m = __builtin_amdgcn_exp2f(mulb<I>(a2, s));
  const float th = __builtin_amdgcn_fractf(mulb<I>(brev, s));
  return (f32x2){m * __builtin_amdgcn_cosf(th), m * __builtin_amdgcn_sinf(th)};
}
template <int CTRL, int RM> DI float addd(float v) {
  return v + __builtin_bit_cast(float, __builtin_amdgcn_update_dpp(0, __builtin_bit_cast(int, v), CTRL, RM, 0xf, true));
}

template <int T0, int T1, int T2, int T3>
DI void conv_tiles(lptr ka, lptr ub, int ust, f32x4 (&acc)[4][4]) {
  constexpr int T[4] = {T0, T1, T2, T3};
  constexpr int NB = 2 * T3 + 2, PF = 8;
  u32x4 af[NB], bfr[4 * NB];
  bool have[NB] = {};
  auto lda = [&](int m) NOPK { af[m] = trd2(ka + m * 256, ka + m * 256 - 32); };
  auto ldb = [&](int n) NOPK {
    lptr q = ub + (n / NB) * ust + (n % NB) * 64;
    bfr[n] = lds128(q);
  };
  auto need = [&](int n) NOPK {
    ldb(n);
    if (n < NB) {
#pragma unroll
      for (int t = 0; t < 4; ++t) { const int m = 2 * T[t] + 1 - n; if (m >= 0 && !have[m]) { lda(m); have[m] = true; } }
    }
  };
  need(0); need(1);
#pragma unroll
  for (int n = 0; n < 4 * NB; ++n) {
    if (n < PF - 2) { need(2 * n + 2); need(2 * n + 3); }
    else if (n + PF < 4 * NB) need(n + PF);
    SCHEDB();
    const int rho = n / NB, be = n % NB;
#pragma unroll
    for (int t = 0; t < 4; ++t) {
      const int m = 2 * T[t] + 1 - be;
      if (m >= 0) acc[t][rho] = mfma16(af[m], bfr[n], be == 0 ? (f32x4){} : acc[t][rho]);
    }
    SCHEDB();
  }
}

#define PFW 1
#define PROG 0
#define UPUB 1
#define ROT ((idx & 7) + 2 * (idx >> 3))
#define DBG_ARGS
#define DBG_PASS
constexpr int XR = 0, KR = 49152, VR = 61440, PHI = 77824, KVI = KR + 8192;
constexpr int CB = 0, CST = 8208;
constexpr int XCH = 0;
constexpr int YS = 16640;
constexpr int RAW = 26112;
constexpr int UT = 36352;
constexpr int UP = 45568, UST = 1168, UC2 = 9344;
constexpr int KL = 64256, KST = 4448;

struct AAddr { u32 xv0, xv1, xv2, xv3, wv0; };
DI AAddr a_addr(int p, int j, int w, int l) {
  AAddr a;
  a.xv0 = (128 * p + 32 * w + (l >> 3)) * 1024 + ((l & 7) ^ (l >> 4)) * 16;
  a.xv1 = (a.xv0 ^ 64) + 7168; a.xv2 = a.xv0 + 2 * 7168; a.xv3 = a.xv1 + 2 * 7168;
  a.wv0 = (l >> 2) * 3072 + j * 64 + (l & 3) * 16;
  return a;
}
DI void role_A(lptr sm, const int b, const char* x, const char* W, const char* wphi, const char* wo, char* out, char* ws DBG_ARGS) {
  const int xcd = b & 7, idx = b >> 3;
  const int p = (xcd >> 1) * 4 + (idx >> 3), j = (xcd & 1) * 8 + (idx & 7);
  const int tid = threadIdx.x;
  const int l = tid & 63, w = __builtin_amdgcn_readfirstlane(tid >> 6);
  const int hh = l >> 5, blk = (l >> 4) & 1, i16 = l & 15, g4 = l >> 4, q4 = i16 >> 2, pp = l & 3, r32 = l & 31;
  const u32 lb = (u32)(size_t)sm;
  const int r0 = ROT;
  const u32x4 sx = mk_srd(x, 2048 * 1024), sw = mk_srd(W, 512 * 3072);
  const AAddr ad = a_addr(p, j, w, l);
#pragma unroll
  for (int c = 0; c < 3; ++c) {
    dma4(lb + XR + c * 16384 + w * 4096, ad.xv0, ad.xv1, ad.xv2, ad.xv3, sx, ((c + r0) & 7) * 128);
    if (c < 2) dma1(lb + KR + c * 4096 + w * 1024, ad.wv0, sw, (((c + r0) & 7) * 64 + 16 * w) * 3072 + 1024);
  }
  dma1(lb + PHI, l * 16, mk_srd(wphi, 32 * 512), j * 1024);
  u32 epoch_;
  asm volatile("s_load_dword %0, %1, 0x0" : "=s"(epoch_) : "s"(ws) : "memory");
  const RSRC rs = __builtin_amdgcn_make_buffer_rsrc(ws, 0, WS_END, 0x00020000);
  const RSRC ro = __builtin_amdgcn_make_buffer_rsrc(out, 0, 4u << 20, 0x00020000);
  STAMPS_DECL; STAMP(1);
  const u32 wv1 = ad.wv0 + 48128, wv2 = ad.wv0 + 2 * 48128, wv3 = ad.wv0 + 3 * 48128;
  auto vslot = [&](int c) NOPK { return c < 7 ? VR + (c & 3) * 4096 : KR + 8192; };
  auto issueW = [&](int dst, int mat, int c) NOPK { dma1(lb + dst + w * 1024, ad.wv0, sw, (((c + r0) & 7) * 64 + 16 * w) * 3072 + mat * 1024); };
  const int rt = xcd * 4 + (idx >> 3), ct = idx & 7;
  const int sh = w & 1, kh = w >> 1, st = 2 * rt + sh;

  const int wlane = (8 * hh + q4) * 64 + blk * 32 + pp * 8;
  const int xsw = (r32 >> 1) & 7;
  u32 epoch;
  lptr vbase = sm + KR + wlane; PIN(vbase);
  u32x4 xf[32], phif[2];
  f32x16 ak = {}, av = {};
#pragma unroll
  for (int c = 0; c < 8; ++c) {
    if (c == 0) WAITV(10); else if (c < 6) WAITV(11); else WAITV(7);
    BAR();
    if (c == 0) { asm volatile("s_mov_b32 %0, %1" : "=s"(epoch) : "s"(epoch_)); STAMP(2); } if (c == 4) STAMP(3); if (c == 7) STAMP(4);
    lptr xs = sm + XR + (c % 3) * 16384 + w * 4096 + r32 * 128;
    lptr wsl = vbase + (c % 3) * 4096;
    lptr vsl = vbase + (vslot(c - 3) - KR);
    u32x4 wk[4], wv[4];
#pragma unroll
    for (int ks = 0; ks < 4; ++ks) { xf[c * 4 + ks] = lds128(xs + (((2 * ks + hh) ^ xsw) * 16)); wk[ks] = trd2(wsl + ks * 1024, wsl + ks * 1024 + 256); }
    if (c + 2 < 8) issueW(KR + ((c + 2) % 3) * 4096, 1, c + 2);
    if (c == 6) { issueW(vslot(6), 2, 6); issueW(vslot(7), 2, 7); }
    SCHEDB();
#pragma unroll
    for (int ks = 0; ks < 4; ++ks) {
      ak = mfma32(wk[ks], xf[c * 4 + ks], ak);
      if (c >= 3) wv[ks] = trd2(vsl + ks * 1024, vsl + ks * 1024 + 256);
      SCHEDB();
    }
    {
      const bool isx = c + 3 < 8, isq = c >= 6;
      const u32 so_ = isx ? (u32)(((c + 3 + r0) & 7) * 128) : (u32)(((4 * (c - 6) + w + r0) & 7) * 64 * 3072);
      if (isx || isq) setm0(lb + XR + ((isx ? c : c - 1) % 3) * 16384 + w * 4096);
      if (c >= 3) av = mfma32(xf[(c - 3) * 4], wv[0], av);
      if (isx) dmao<0>(ad.xv0, sx, so_); else if (isq) dmao<0>(ad.wv0, sw, so_);
      SCHEDB();
      if (c >= 3) av = mfma32(xf[(c - 3) * 4 + 1], wv[1], av);
      if (isx) dmao<1024>(ad.xv1, sx, so_); else if (isq) dmao<1024>(wv1, sw, so_);
      SCHEDB();
      if (c >= 3) av = mfma32(xf[(c - 3) * 4 + 2], wv[2], av);
      if (isx) dmao<2048>(ad.xv2, sx, so_); else if (isq) dmao<2048>(wv2, sw, so_);
      SCHEDB();
      if (c >= 3) av = mfma32(xf[(c - 3) * 4 + 3], wv[3], av);
      if (isx) dmao<3072>(ad.xv3, sx, so_); else if (isq) dmao<3072>(wv3, sw, so_);
      if (c < 6) issueW(vslot(c), 2, c);
    }
    if (c == 2) {
      const u32 sg = ((l >> 2) & 1) ? 0x80008000u : 0u;
      lptr pb = sm + PHI + (4 * hh + q4) * 32 + (8 * blk + 4 * (pp >> 1)) * 2;
#pragma unroll
      for (int h = 0; h < 2; ++h) { u32x4 v = trd2(pb + h * 512, pb + h * 512 + 256); phif[h] = (u32x4){v.x ^ sg, v.y ^ sg, v.z ^ sg, v.w ^ sg}; }
    }
    SCHEDB();
  }
  STAMP(5);
  const u32 tag = mk_tag(epoch);
  const u32x2 ftag = {tag, mk_tag2(epoch)};
  const u32x4 so = mk_srd(wo, 512 * 1024);
  const u32 ov0 = ((l >> 2) & 7) * 1024 + ct * 128 + hh * 64 + pp * 16;
  const u32 ov1 = ov0 + 7168, ov2 = ov0 + 2 * 7168, ov3 = ov0 + 3 * 7168;
  constexpr int WOB[4] = {VR, 16384, 32768, 0};
  {
    constexpr int PT = XR + 16384, VT = KR;
    const f32x16 zero = {};
    f32x16 pk[2];
    pk[0] = mfma32(phif[0], pack8(ak, 0), zero); pk[1] = mfma32(phif[1], pack8(ak, 8), zero);
    lptr tb = sm + PT + w * 4096 + l * 32;
    {
      const char* t = l < 32 ? wo + (idx * 128 + w * 32 + l) * 128 : out + (size_t)(l < 48 ? 64 * rt : 64 * idx) * 2048;
      asm volatile("s_mov_b32 m0, %0\n\ts_nop 0\n\tglobal_load_lds_dword %1, off" :: "s"(lb + PHI + 1024), "v"(t) : "memory");
    }
    WAITV(9);
    BAR();
    STAMP(6);
    {
      u32x4 vf[12];
      auto ldv = [&](int i) NOPK { lptr q = vbase + (vslot(5 + (i >> 2)) - KR) + (i & 3) * 1024; vf[i] = trd2(q, q + 256); };
#pragma unroll
      for (int i = 0; i < 6; ++i) ldv(i);
      SCHEDB();
      u32x4 lo, hi;
      softmax16h(pk[0], lo, hi, [&](auto ii) NOPK {
        constexpr int i = decltype(ii)::v;
        SCHEDB();
        av = mfma32(xf[20 + i], vf[i], av);
        if constexpr (i + 6 < 12) ldv(i + 6);
        SCHEDB();
      });
      *(LAS u32x4*)tb = lo; *(LAS u32x4*)(tb + 16) = hi;
      softmax16h(pk[1], lo, hi, [&](auto ii) NOPK {
        constexpr int i = decltype(ii)::v;
        if constexpr (i < 2) { SCHEDB(); av = mfma32(xf[28 + 2 * i], vf[8 + 2 * i], av); av = mfma32(xf[29 + 2 * i], vf[9 + 2 * i], av); SCHEDB(); }
        if constexpr (i == 4) { SCHEDB(); *(LAS u32x4*)(sm + VT + w * 2048 + l * 16) = pack8(av, 0); SCHEDB(); }
        if constexpr (i == 5) { SCHEDB(); *(LAS u32x4*)(sm + VT + w * 2048 + 1024 + l * 16) = pack8(av, 8); SCHEDB(); }
        if constexpr (i == 6) { SCHEDB(); *(LAS u32x4*)(tb + 2048) = lo; SCHEDB(); }
      });
      *(LAS u32x4*)(tb + 2064) = hi;
    }
    STAMP(7);
    waitv<PFW>();
    BAR();
    STAMP(8);
    lptr ta = sm + PT + w * 1024 + (16 * hh + 4 * blk + q4) * 32 + pp * 8;
    lptr vb = sm + VT + hh * 1024 + (16 * (w >> 1) + i16 + 32 * blk) * 16;
    const u32 one = 0x3F803F80u;
    f32x4 kv = {}, kz = {};
#pragma unroll
    for (int s4 = 0; s4 < 4; ++s4) {
      const u32x4 a = trd2(ta + s4 * 4096, ta + s4 * 4096 + 256);
      kv = mfma16(a, lds128(vb + s4 * 2048), kv);
      kz = mfma16(a, (u32x4){one, one, one, one}, kz);
    }
    const u32 pbase = WS_KVP + (j * 16 + p) * KVS;
    st64c(rs, pbase + w * 512 + l * 8, (u32x2){pk2(kv[0], kv[1]), pk2(kv[2], kv[3])});
    st64c(rs, i16 == 0 ? pbase + 2048 + w * 32 + g4 * 8 : OOB, (u32x2){pk2(kz[0], kz[1]), pk2(kz[2], kz[3])});
    MEMB(); SCHEDB();
  }
  STAMP(9);
  f32x16 aq = {};
  {
    u32x4 wq[2][4];
    auto ldq = [&](int c) NOPK {
      lptr wsl = sm + XR + ((c < 4) ? 32768 : 0) + (c & 3) * 4096 + wlane;
#pragma unroll
      for (int ks = 0; ks < 4; ++ks) wq[c & 1][ks] = trd2(wsl + ks * 1024, wsl + ks * 1024 + 256);
    };
    ldq(0);
#pragma unroll
    for (int c = 0; c < 8; ++c) {
      if (c < 7) ldq(c + 1);
      SCHEDB();
#pragma unroll
      for (int ks = 0; ks < 4; ++ks) aq = mfma32(wq[c & 1][ks], xf[c * 4 + ks], aq);
      SCHEDB();
      if (c == KVFC) { WAITV(0); st64c(rs, l == 0 ? WS_KVF + ((j * 4 + w) * 16 + p) * 128 : OOB, ftag); STAMP(10); }
      if (c == 6) BAR();
    }
  }
  STAMP(11);
  auto issueO = [&](int i) NOPK { dma4(lb + WOB[i] + w * 4096, ov0, ov1, ov2, ov3, so, (128 * w + 32 * i) * 1024); };
  u32x4 pq[2][2], sel[2];
  const u32 kfo = WS_KVF + ((j * 4 + w) * 16 + i16) * 128;
  {
    const f32x16 zero = {};
    issueO(0);
    const f32x16 t0 = mfma32(phif[0], pack8(aq, 0), zero), t1 = mfma32(phif[1], pack8(aq, 8), zero);
    SCHEDB();
    issueO(1);
    const u32 onev = (l & 1) ? 0x3F800000u : 0x00003F80u;
#pragma unroll
    for (int cg = 0; cg < 2; ++cg)
#pragma unroll
      for (int d = 0; d < 4; ++d) sel[cg][d] = ((i16 >> 1) == 4 * cg + d) ? onev : 0u;
    PIN(sel[0]); PIN(sel[1]);
    issueO(2);
    softmax16(t0, pq[0][0], pq[0][1]); PIN(pq[0][0]); PIN(pq[0][1]);
    issueO(3);
    const u32x2 f0 = ld64c(rs, kfo);
    MEMB(); SCHEDB();
    softmax16(t1, pq[1][0], pq[1][1]); PIN(pq[1][0]); PIN(pq[1][1]);
    SCHEDB();
    STAMP(12);
    if (!flag_ok(f0, ftag)) wait_flags(rs, kfo, ftag);
    MEMB();
  }
  STAMP(13);
  {
    const u32 base = WS_KVP + j * 16 * KVS;
    u32x4 v[8];
#pragma unroll
    for (int i = 0; i < 8; ++i) v[i] = ld128c(rs, base + (4 * (i >> 1) + g4) * KVS + w * 512 + (i & 1) * 256 + i16 * 16);
    const u32x2 zv = ld64c(rs, base + (l >> 2) * KVS + 2048 + w * 32 + pp * 8);
    MEMB(); SCHEDB();
    f32x4 a = {};
#pragma unroll
    for (int i = 0; i < 8; ++i) a = mfma16(sel[i & 1], v[i], a);
    *(LAS u32x2*)(sm + KVI + w * 1024 + (pp >> 1) * 512 + (16 + (l >> 2)) * 16 + (pp & 1) * 8) = zv;
    *(LAS u32x2*)(sm + KVI + w * 1024 + hh * 512 + (2 * (l & 7) + blk) * 16 + (i16 >> 3) * 8) = (u32x2){pk2(a[0], a[1]), pk2(a[2], a[3])};
  }
  WAITV(0);
  BAR();
  STAMP(14);
  u32x4 wf[32];
  {
    const f32x16 zero = {};
    lptr ap = sm + KVI + hh * 1024 + r32 * 16;
    f32x16 t[2];
#pragma unroll
    for (int h = 0; h < 2; ++h) { t[h] = mfma32(lds128(ap + h * 2048), pq[h][0], zero); t[h] = mfma32(lds128(ap + h * 2048 + 512), pq[h][1], t[h]); }
#pragma unroll
    for (int h = 0; h < 2; ++h) {
      const float dh = ((t[h][8] + t[h][9]) + (t[h][10] + t[h][11])) + ((t[h][12] + t[h][13]) + (t[h][14] + t[h][15]));
      const auto sw2 = __builtin_amdgcn_permlane32_swap(f2u(dh), f2u(dh), false, false);
      const float r = __builtin_amdgcn_rcpf((u2f(sw2[0]) + u2f(sw2[1])) + 1e-6f);
      st128c(rs, WS_OFR + ((4 * p + w) * 32 + 2 * j + h) * 1024 + l * 16,
             (u32x4){pk2(t[h][0] * r, t[h][1] * r), pk2(t[h][2] * r, t[h][3] * r), pk2(t[h][4] * r, t[h][5] * r), pk2(t[h][6] * r, t[h][7] * r)});
    }
    MEMB(); SCHEDB();
    STAMP(15);
    lptr wb = sm + kh * 8192 + (4 * hh + q4) * 64 + blk * 32 + pp * 8;
    auto ldw = [&](int i) NOPK {
      lptr q = wb + WOB[(i >> 1) & 3] + (i >> 3) * 4096 + (i & 1) * 2048;
      wf[2 * i] = trd2(q, q + 1024); wf[2 * i + 1] = trd2(q + 512, q + 1536);
    };
#pragma unroll
    for (int i = 0; i < 16; ++i) ldw(i);
    WAITV(0);
    st64c(rs, l == 0 ? WS_OF + ((4 * p + w) * 16 + j) * 128 : OOB, ftag);
  }
  BAR();
  STAMP(16);
  if (OPRE) __builtin_amdgcn_s_sleep(OPRE);
  f32x16 acc0 = {}, acc1 = {};
  {
    const u32 obase = WS_OFR + (st * 32 + 16 * kh) * 1024 + l * 16;
    const u32 fo = WS_OF + (st * 16 + 8 * kh + (l & 7)) * 128;
    u32x4 of[16];
    wait_flags(rs, fo, ftag);
    STAMP(18);
#pragma unroll
    for (int i = 0; i < 16; ++i) of[i] = ld128c(rs, obase + i * 1024);
    MEMB(); SCHEDB();
#pragma unroll
    for (int i = 0; i < 16; ++i) { acc0 = mfma32(wf[2 * i], of[i], acc0); acc1 = mfma32(wf[2 * i + 1], of[i], acc1); }
  }
  MEMB(); SCHEDB();
  STAMP(19);
  {
    lptr rp = sm + kh * 17408 + (32 * sh + r32) * 272 + hh * 16;
#pragma unroll
    for (int g = 0; g < 4; ++g) *(LAS f32x4*)(rp + g * 32) = (f32x4){acc0[4 * g], acc0[4 * g + 1], acc0[4 * g + 2], acc0[4 * g + 3]};
#pragma unroll
    for (int g = 0; g < 4; ++g) *(LAS f32x4*)(rp + 128 + g * 32) = (f32x4){acc1[4 * g], acc1[4 * g + 1], acc1[4 * g + 2], acc1[4 * g + 3]};
    BAR();
#pragma unroll
    for (int i = 0; i < 2; ++i) {
      const int row = i * 32 + (tid >> 3), oc = tid & 7;
      lptr sp = sm + row * 272 + oc * 32;
      const f32x4 a = *(LAS f32x4*)sp + *(LAS f32x4*)(sp + 17408), bb = *(LAS f32x4*)(sp + 16) + *(LAS f32x4*)(sp + 17408 + 16);
      __builtin_amdgcn_raw_buffer_store_b128((u32x4){pk2(a[0], a[1]), pk2(a[2], a[3]), pk2(bb[0], bb[1]), pk2(bb[2], bb[3])}, ro, (64 * rt + row) * 2048 + ct * 128 + oc * 16, 0, OAUX);
    }
  }
  st32c(rs, (b == 0 && tid == 0) ? WS_EPOCH : OOB, epoch + 1);
  STAMP(20);
  STAMPS_OUT();
}

DI void prologue_B(u32 lb, const int b, const char* u, const char* Are, const char* Aim, const char* Bre, const char* Bim, const char* Cre, const char* Cim) {
  const int gam = b & 7, idx = b >> 3;
  const int l = threadIdx.x & 63, w = __builtin_amdgcn_readfirstlane(threadIdx.x >> 6);
  const int ar = l >> 3;
  const char* pa = ar < 2 ? (ar ? Aim : Are) : (ar < 4 ? ((ar & 1) ? Bim : Bre) : ((ar & 1) ? Cim : Cre));
  pa += (64 * gam + 2 * idx) * 256 + w * 128 + (l & 7) * 16;
  asm volatile("s_mov_b32 m0, %0\n\ts_nop 0\n\tglobal_load_lds_dwordx4 %1, off" :: "s"(lb + RAW + w * 1024), "v"(pa) : "memory");
  const u32 uo = (l >> 3) * 1024 + (((l & 7) ^ (2 * (l >> 5))) * 16);
  setm0(lb + UT + w * 2048);
  const u32x4 su = mk_srd(u, 2048 * 1024);
  const u32 so = (64 * idx + 16 * w) * 1024 + gam * 128;
  dmao<0>(uo, su, so); dmao<1024>((uo ^ 64) + 7168, su, so);
}

DI void role_B(lptr sm, const int b, const char* ldt, const char* Dsk, char* out, char* ws DBG_ARGS) {
  const int gam = b & 7, idx = b >> 3;
  const int tid = threadIdx.x;
  const int l = tid & 63, w = __builtin_amdgcn_readfirstlane(tid >> 6);
  const int g4 = l >> 4, i16 = l & 15, q4 = i16 >> 2, pp = l & 3, c = w >> 1, hs = w & 1;
  const RSRC rs = __builtin_amdgcn_make_buffer_rsrc(ws, 0, WS_END, 0x00020000);
  const RSRC ro = __builtin_amdgcn_make_buffer_rsrc(out, 0, 4u << 20, 0x00020000);
  STAMPS_DECL; STAMP(1);
  u32 epoch_, ld2_, dk2_;
  asm volatile("s_load_dword %0, %3, %4\n\ts_load_dword %1, %5, %7\n\ts_load_dword %2, %6, %7"
               : "=&s"(epoch_), "=&s"(ld2_), "=&s"(dk2_) : "s"(ws), "s"(WS_EPOCH + 128 * (1 + gam)), "s"(ldt), "s"(Dsk), "s"((64 * gam + 2 * idx) * 2) : "memory");
  lptr kb = sm + KL + c * KST + 32;
  lptr dummy = sm + KL + 2 * KST + l * 16;
  const int beta = i16 < 8 ? i16 : (i16 < 12 ? 240 + 2 * i16 : 128 * (i16 & 14) - 1024 + 4 * (i16 & 1));
  lptr zp = tid < 128 ? sm + UP + (tid >> 3) * UST + 16 + (tid & 7) * 16 : (tid < 132 ? sm + KL + ((tid >> 1) & 1) * KST + (tid & 1) * 16 : dummy);
  *(LAS u32x4*)zp = (u32x4){0, 0, 0, 0};
  u32 epoch, ld2, dk2;
  asm volatile("s_waitcnt vmcnt(2) lgkmcnt(0)\n\ts_mov_b32 %0, %3\n\ts_mov_b32 %1, %4\n\ts_mov_b32 %2, %5"
               : "=&s"(epoch), "=&s"(ld2), "=&s"(dk2) : "s"(epoch_), "s"(ld2_), "s"(dk2_) : "memory");
  STAMP(2);
  const u32 tag = mk_tag(epoch);
  const u32x2 ftag = {tag, mk_tag2(epoch)};
  float a2, brev, ksum; f32x2 cc, m0, m1;
  {
    lptr rp = sm + RAW + w * 1024 + l * 2;
    const float are = bf1(*(LAS u16*)rp), aim = bf1(*(LAS u16*)(rp + 128)), bre = bf1(*(LAS u16*)(rp + 256)), bim = bf1(*(LAS u16*)(rp + 384));
    const float cre = bf1(*(LAS u16*)(rp + 512)), cim = bf1(*(LAS u16*)(rp + 640));
    const float dt = rbf(__builtin_amdgcn_exp2f((c ? bf_hi(ld2) : bf_lo(ld2)) * 1.4426950408889634f));
    cc = cmul((f32x2){cre, cim}, (f32x2){dt * bre, dt * bim});
    a2 = (dt * are) * 1.4426950408889634f; brev = (dt * aim) * 0.15915494309189535f;
    m0 = cexp_s(a2, brev, 128.f); m1 = cexp_s(a2, brev, 1024.f);
    const f32x2 g = cmul(cc, m1);
    ksum = __builtin_fmaf(-g.y, m1.y, g.x * m1.x);
    ksum = addd<0x111, 0xf>(ksum); ksum = addd<0x112, 0xf>(ksum); ksum = addd<0x114, 0xf>(ksum); ksum = addd<0x118, 0xf>(ksum);
    ksum = addd<0x142, 0xa>(ksum); ksum = addd<0x143, 0xc>(ksum);
    PIN(a2); PIN(brev); PIN(cc); PIN(m0); PIN(m1); PIN(ksum);
  }
  MEMB(); SCHEDB();
  STAMP(3);
  {
    f32x4 dA = {}, dB = {}, dC = {}, dD = {};
    const float sA = (float)(8 * i16), sB = (float)beta;
    const u32 ufo = uf_off(gam + 8 * (8 * w + (l & 7)), idx >> 3);
    u32x2 f0;
    auto kgen = [&](auto kk) NOPK {
      constexpr int ks = decltype(kk)::v;
      u32x4 o1, o2, b1, b2;
      auto elem = [&](auto ee) NOPK {
        constexpr int e = decltype(ee)::v, I = 4 * ks + e;
        const f32x2 ea_ = cexp_b<I>(a2, brev, sA), eb = cexp_b<I>(a2, brev, sB);
        const f32x2 v1 = {fnmab<I>(cc.y, ea_.y, mulb<I>(cc.x, ea_.x)), fnmab<I>(cc.y, ea_.x, mulb<I>(cc.x, -ea_.y))};
        const f32x2 v2 = {fmab<I>(m0.y, v1.y, mulb<I>(m0.x, v1.x)), fnmab<I>(m0.y, v1.x, mulb<I>(m0.x, v1.y))};
        const f32x2 w2 = {fnmab<I>(m1.y, eb.y, mulb<I>(m1.x, eb.x)), fmab<I>(m1.y, eb.x, mulb<I>(m1.x, eb.y))};
        o1[e] = pk2(v1.x, v1.y); o2[e] = pk2(v2.x, v2.y); b1[e] = pk2(eb.x, eb.y); b2[e] = pk2(w2.x, w2.y);
      };
      elem(IC<0>{}); elem(IC<1>{}); elem(IC<2>{}); elem(IC<3>{});
      dA = mfma16(b1, o1, dA); dB = mfma16(b1, o2, dB); dC = mfma16(o1, b2, dC); dD = mfma16(o2, b2, dD);
      SCHEDB();
      if constexpr (ks == UPUB - 1) {
        STAMP(4);
        WAITV(0); BAR();
        u32x4 tv[2];
#pragma unroll
        for (int e = 0; e < 2; ++e) {
          lptr a = sm + UT + (32 * (g4 >> 1) + 4 * q4 + 2 * e + (g4 & 1)) * 128 + (((2 * w + (pp >> 1)) ^ (2 * q4)) * 16) + (pp & 1) * 8;
          tv[e] = trd2(a, a + 16 * 128);
        }
#pragma unroll
        for (int e = 0; e < 2; ++e) *(LAS u32x4*)(sm + YS + (16 * w + i16) * 144 + (4 * e + 2 * (g4 & 1) + (g4 >> 1)) * 16) = tv[e];
#pragma unroll
        for (int e = 0; e < 2; ++e)
          st128c(rs, WS_UX + (64 * gam + 16 * w + 8 * e + (l >> 3)) * 4096 + idx * 128 + (l & 7) * 16, lds128(sm + YS + (16 * w + 8 * e + (l >> 3)) * 144 + (l & 7) * 16));
        MEMB(); SCHEDB();
      }
      if constexpr (ks == UPUB) { WAITV(0); st64c(rs, l == 0 ? uf_off(b, w) : OOB, ftag); STAMP(5); }
    };
    kgen(IC<0>{}); kgen(IC<1>{}); kgen(IC<2>{}); kgen(IC<3>{});
    dD[3] = l == 63 ? ksum : dD[3];
    STAMP(6);
    __builtin_amdgcn_s_setprio(BP1);
    f0 = ld64c(rs, ufo);
    const bool first = (i16 == 0) && (hs == 0);
    lptr ea = kb + (8 * i16 + 128 * hs) * 2;
    lptr e1 = ea + (g4 < 2 ? g4 * 8 : (g4 - 1) * 512);
    lptr e2 = g4 < 2 ? dummy : e1 + 8;
    lptr e3 = g4 < 2 ? dummy : (g4 == 2 ? e1 + 12 : e1 - 2);
    lptr e4 = (g4 < 2 || first) ? dummy : (g4 == 2 ? e1 - 2 : e1 - 4);
    lptr e5 = g4 == 3 ? e1 + 512 : dummy + 8;
    const bool rv = (beta & 255) == 0;
    lptr r1 = rv ? kb + (1024 + beta + 128 * hs + 32 * g4) * 2 : dummy + 16;
    lptr r0 = (l == 0 && hs == 0) ? dummy : r1 - 8;
    lptr r2 = (l == 63 && hs == 1) ? kb + 2044 * 2 : r1 + 48;
    PIN(e1); PIN(e2); PIN(e3); PIN(e4); PIN(e5); PIN(r0); PIN(r1); PIN(r2);
    lptr xc = sm + XCH + l * 16;
    *(LAS f32x4*)(xc + w * 1024) = hs ? dA : dB; *(LAS f32x4*)(xc + w * 1024 + 4096) = hs ? dC : dD;
    BAR();
    f32x4 dt_ = (hs ? dB : dA) + *(LAS f32x4*)(xc + (w ^ 1) * 1024);
    const f32x4 dr = (hs ? dD : dC) + *(LAS f32x4*)(xc + (w ^ 1) * 1024 + 4096);
    STAMP(7);
    if (!flag_ok(f0, ftag)) wait_flags(rs, ufo, ftag);
    MEMB();
    u32x4 uv[2];
#pragma unroll
    for (int e = 0; e < 2; ++e) uv[e] = ld128c(rs, WS_UX + (64 * gam + 2 * idx + e) * 4096 + tid * 16);
    MEMB(); SCHEDB();
    STAMP(8);
    dt_[0] += (l == 0 && hs == 0) ? (c ? bf_hi(dk2) : bf_lo(dk2)) : 0.f;
    {
      u32 p00 = pk2(dt_[0], dt_[0]), p01 = pk2(dt_[0], dt_[1]), p11 = pk2(dt_[1], dt_[1]), p12 = pk2(dt_[1], dt_[2]);
      u32 p22 = pk2(dt_[2], dt_[2]), p23 = pk2(dt_[2], dt_[3]), p33 = pk2(dt_[3], dt_[3]);
      PIN(p00); PIN(p01); PIN(p11); PIN(p12); PIN(p22); PIN(p23); PIN(p33);
      const u32 t2 = g4 == 2 ? p12 : p01;
      *(LAS u32x2*)e1 = (u32x2){g4 < 2 ? p01 : p00, g4 < 2 ? p23 : t2};
      *(LAS u32*)e2 = g4 == 2 ? p22 : p11;
      *(LAS u16*)e3 = (u16)(g4 == 2 ? p33 : p00);
      *(LAS u16*)e4 = (u16)p00;
      *(volatile LAS u32*)(e5 - 4) = p22; *(volatile LAS u32x2*)e5 = (u32x2){p22, p23}; *(volatile LAS u32*)(e5 + 8) = p33;
    }
    {
      const u32 p00 = pk2(dr[0], dr[0]), p11 = pk2(dr[1], dr[1]), p22 = pk2(dr[2], dr[2]), p33 = pk2(dr[3], dr[3]);
      *(LAS u32*)r0 = p00;
      *(volatile LAS u32*)(r1 - 4) = p00;
      *(LAS u32x4*)r1 = (u32x4){p00, p00, pk2(dr[0], dr[1]), p11};
      *(LAS u32x4*)(r1 + 16) = (u32x4){p11, p11, p22, p22};
      *(LAS u32x4*)(r1 + 32) = (u32x4){p22, p22, pk2(dr[2], dr[3]), p33};
      *(LAS u32x2*)r2 = (u32x2){p33, p33};
    }
    MEMB(); SCHEDB();
    STAMP(9);
#pragma unroll
    for (int e = 0; e < 2; ++e) {
      lptr q2 = sm + UP + (e * 4 + ((tid >> 1) & 3)) * UST + 128 + (tid >> 3) * 32 + (tid & 1) * 16;
      *(LAS u32x4*)q2 = uv[e];
      *(volatile LAS u32x2*)(q2 + UC2 + 8) = (u32x2){uv[e].x, uv[e].y};
      *(volatile LAS u32x2*)(q2 + UC2 + 16) = (u32x2){uv[e].z, uv[e].w};
    }
  }
  BAR();
  *(LAS f32x4*)(tid < 2 ? sm + CB + tid * CST : dummy) = (f32x4){};
  st32c(rs, (idx == 0 && tid == 0) ? WS_EPOCH + 128 * (1 + gam) : OOB, epoch + 1);
  STAMP(10);
  __builtin_amdgcn_s_setprio(BP1);
  {
    f32x4 acc[4][4];
    lptr ka = kb + (4 * pp - 4 * (8 * g4 + q4) + 112) * 2;
    lptr ub = sm + UP + c * 4 * UST + 128 + (4 * i16 + 8 * g4 - 60) * 2 + ((l & 1) ? 0 : UC2 + 8);
    if (hs == 0) conv_tiles<0, 3, 4, 7>(ka, ub, UST, acc); else conv_tiles<1, 2, 5, 6>(ka, ub, UST, acc);
    STAMP(11);
    __builtin_amdgcn_s_setprio(BP2);
    lptr cb = sm + CB + c * CST + 16;
    f32x4 y[4];
#pragma unroll
    for (int t = 0; t < 4; ++t) {
      const int tau = 2 * t + ((t & 1) ^ hs);
      const f32x4 *z = acc[t];
      *(LAS f32x4*)(cb + (64 * tau + l) * 16) = (f32x4){(z[1][3] + z[2][2]) + z[3][1], z[2][3] + z[3][2], z[3][3], z[3][3]};
    }
#pragma unroll
    for (int t = 0; t < 4; ++t) {
      const f32x4 *z = acc[t];
      y[t] = (f32x4){z[0][0], z[0][1] + z[1][0], (z[0][2] + z[1][1]) + z[2][0], (z[0][3] + z[1][2]) + (z[2][1] + z[3][0])};
      PIN(y[t]);
    }
    BAR();
    STAMP(12);
    const int src = g4 > 0 ? l - 16 : (i16 > 0 ? 47 + i16 : -1);
    u32x2 yp[4];
#pragma unroll
    for (int t = 0; t < 4; ++t) {
      const int tau = 2 * t + ((t & 1) ^ hs);
      const f32x4 r = *(LAS f32x4*)(cb + (64 * tau + src) * 16);
      yp[t] = (u32x2){pk2(y[t][0] + r[0], y[t][1] + r[1]), pk2(y[t][2] + r[2], y[t][3])};
    }
#pragma unroll
    for (int t = 0; t < 4; t += 2) {
      const auto s0 = __builtin_amdgcn_permlane16_swap(yp[t].x, yp[t + 1].x, false, false);
      const auto s1 = __builtin_amdgcn_permlane16_swap(yp[t].y, yp[t + 1].y, false, false);
      const int tau = (g4 & 1) ? 2 * t + 3 - hs : 2 * t + hs;
      st128c(rs, WS_YT + (64 * gam + 2 * idx + c) * 4096 + tau * 512 + q4 * 128 + (g4 >> 1) * 64 + pp * 16, (u32x4){s0[0], s1[0], s0[1], s1[1]});
    }
  }
  MEMB(); SCHEDB();
  STAMP(13);
  WAITV(0);
  st64c(rs, l == 0 ? WS_YF + ((gam * 4 + w) * 32 + idx) * 128 : OOB, ftag);
  STAMP(14);
  __builtin_amdgcn_s_sleep(YPRE);
  {
    const int tl = idx >> 2, hsn = ((tl >> 1) ^ tl) & 1;
    wait_flags(rs, WS_YF + ((gam * 4 + 2 * ((l >> 3) & 1) + hsn) * 32 + 8 * w + (l & 7)) * 128, ftag);
  }
  STAMP(15);
  {
    u32x4 v[2];
#pragma unroll
    for (int e = 0; e < 2; ++e) v[e] = ld128c(rs, WS_YT + (64 * gam + 16 * w + 8 * e + (l >> 3)) * 4096 + idx * 128 + (l & 7) * 16);
#pragma unroll
    for (int e = 0; e < 2; ++e) *(LAS u32x4*)(sm + YS + (16 * w + 8 * e + (l >> 3)) * 144 + (2 * pp + ((l >> 2) & 1)) * 16) = v[e];
  }
  BAR();
#pragma unroll
  for (int e = 0; e < 2; ++e) {
    lptr a = sm + YS + (8 * (4 * e + g4) + q4) * 144 + w * 32 + pp * 8;
    *(LAS u32x4*)(sm + UT + (16 * w + i16) * 144 + (4 * e + g4) * 16) = trd2(a, a + 4 * 144);
  }
#pragma unroll
  for (int e = 0; e < 2; ++e)
    __builtin_amdgcn_raw_buffer_store_b128(lds128(sm + UT + (16 * w + 8 * e + (l >> 3)) * 144 + (l & 7) * 16), ro, (64 * idx + 16 * w + 8 * e + (l >> 3)) * 2048 + 1024 + gam * 128 + (l & 7) * 16, 0, OAUX);
  STAMP(16);
  STAMPS_OUT();
}

#define RUN_A 1
#define RUN_B 1
__global__ __launch_bounds__(256, 2) NOPK void fused_kernel(const char* x, const char* W, const char* wphi, const char* wo, const char* u,
                                                            const char* Are, const char* Aim, const char* Bre, const char* Bim, const char* Cre, const char* Cim,
                                                            const char* ldt, const char* Dsk, char* out, char* ws) {
  extern __shared__ __attribute__((aligned(16))) char smem[];
  lptr sm = (lptr)smem;
  asm volatile("" :: "s"(x), "s"(W), "s"(wphi), "s"(wo), "s"(u), "s"(Are), "s"(Aim), "s"(Bre), "s"(Bim), "s"(Cre), "s"(Cim), "s"(ldt), "s"(Dsk), "s"(out), "s"(ws));
  const int b = blockIdx.x & 255;
  u32 isA = blockIdx.x >> 8;
  __builtin_amdgcn_s_setprio(3);
  if (!isA) prologue_B((u32)(size_t)sm, b, u, Are, Aim, Bre, Bim, Cre, Cim);
  asm volatile("" : "+s"(isA));
  if (__builtin_expect(isA, 1)) { __builtin_amdgcn_s_setprio(APRIO); role_A(sm, b, x, W, wphi, wo, out, ws DBG_PASS); }
  else { __builtin_amdgcn_s_setprio(BP0); role_B(sm, b, ldt, Dsk, out, ws DBG_PASS); }
}

extern "C" void kernel_launch(void* const* d_in, const int* in_sizes, int n_in, void* d_out, int out_size, void* d_ws, size_t ws_size,
                              hipStream_t stream) {
  (void)hipFuncSetAttribute((const void*)fused_kernel, hipFuncAttributeMaxDynamicSharedMemorySize, LDS_BYTES);
  fused_kernel<<<512, 256, LDS_BYTES, stream>>>((const char*)d_in[0], (const char*)d_in[1], (const char*)d_in[2], (const char*)d_in[3], (const char*)d_in[4],
                                                (const char*)d_in[5], (const char*)d_in[6], (const char*)d_in[7], (const char*)d_in[8], (const char*)d_in[9], (const char*)d_in[10],
                                                (const char*)d_in[11], (const char*)d_in[12], (char*)d_out, (char*)d_ws);
}
```

```cpp
#include <hip/hip_runtime.h>
#include <hip/hip_bf16.h>

typedef unsigned int u32;
typedef unsigned long long u64;
typedef unsigned short u16;
typedef short s16x4 __attribute__((ext_vector_type(4)));
typedef short s16x8 __attribute__((ext_vector_type(8)));
typedef __bf16 bf16x2 __attribute__((ext_vector_type(2)));
typedef __bf16 bf16x8 __attribute__((ext_vector_type(8)));
typedef float f32x2 __attribute__((ext_vector_type(2)));
typedef float f32x4 __attribute__((ext_vector_type(4)));
typedef float f32x16 __attribute__((ext_vector_type(16)));
typedef u32 u32x2 __attribute__((ext_vector_type(2)));
typedef u32 u32x4 __attribute__((ext_vector_type(4)));
#define LAS __attribute__((address_space(3)))
#define GAS __attribute__((address_space(1)))
#if defined(__HIP_DEVICE_COMPILE__)
#define NOPK __attribute__((target("no-packed-fp32-ops")))
#else
#define NOPK
#endif
#define DI __device__ __forceinline__ NOPK
typedef LAS char* lptr;

#define PSLEEP 16
#define OPRE 20
#define YPRE 20
#define UFK 1
#define APRIO 2
#define BP0 0
#define BP1 0
#define BP2 0
#define ADELAY 2
#define CONVT 0
#define KVFC 4
#define KSPLIT 3
#define OAUX 17
#define PACE1 0
#define PACE2 0
#define PACE_SLEEP 2
#define PACE1C 7
#define SWAP_ROLES 1
constexpr u32 WS_EPOCH = 0;
constexpr u32 WS_OF = 4096;
constexpr u32 WS_UF = WS_OF + 1024 * 128;
constexpr u32 WS_YF = WS_UF + 1024 * 128;
constexpr u32 WS_KVF = WS_YF + 1024 * 128;
constexpr u32 WS_PF = WS_KVF + 1024 * 128;
constexpr u32 WS_KVP = 1u << 20;
constexpr u32 KVS = 2176;
constexpr u32 WS_OFR = 2u << 20;
constexpr u32 WS_UX = 4u << 20;
constexpr u32 WS_YT = 6u << 20;
constexpr u32 WS_END = 8u << 20;
constexpr u32 OOB = 0x7FFFFFF0u;
constexpr int LDS_BYTES = 81920;
constexpr u32 SPIN_CAP = 1u << 16;

#define STAMPS_DECL
#define STAMP(k)
#define STAMPS_OUT()

#define BAR() asm volatile("s_waitcnt lgkmcnt(0)\n\ts_barrier" ::: "memory")
#define WAITV(n) asm volatile("s_waitcnt vmcnt(" #n ")" ::: "memory")
#define MEMB() asm volatile("" ::: "memory")
#define SCHEDB() __builtin_amdgcn_sched_barrier(0)

DI float u2f(u32 v) { return __builtin_bit_cast(float, v); }
DI u32 f2u(float v) { return __builtin_bit_cast(u32, v); }
DI bool all_lanes(bool c) { return __builtin_amdgcn_ballot_w64(!c) == 0; }
DI u32 pk2(float a, float b) { f32x2 v = {a, b}; return __builtin_bit_cast(u32, __builtin_convertvector(v, bf16x2)); }
DI float bf_lo(u32 v) { return u2f(v << 16); }
DI float bf_hi(u32 v) { return u2f(v & 0xffff0000u); }
DI float bf1(u16 v) { return u2f((u32)v << 16); }
DI float rbf(float a) { return bf_lo(pk2(a, a)); }
DI u32x2 trd(lptr p) { return __builtin_bit_cast(u32x2, __builtin_amdgcn_ds_read_tr16_b64_v4i16((LAS s16x4*)p)); }
DI u32x4 trd2(lptr p0, lptr p1) { u32x2 a = trd(p0), b = trd(p1); return (u32x4){a.x, a.y, b.x, b.y}; }
DI u32x4 lds128(lptr p) { return *(LAS u32x4*)p; }
DI u32x2 lds64(lptr p) { return *(volatile LAS u32x2*)p; }
DI f32x16 mfma32(u32x4 a, u32x4 b, f32x16 c) { return __builtin_amdgcn_mfma_f32_32x32x16_bf16(__builtin_bit_cast(bf16x8, a), __builtin_bit_cast(bf16x8, b), c, 0, 0, 0); }
DI f32x4 mfma16(u32x4 a, u32x4 b, f32x4 c) { return __builtin_amdgcn_mfma_f32_16x16x32_bf16(__builtin_bit_cast(bf16x8, a), __builtin_bit_cast(bf16x8, b), c, 0, 0, 0); }
DI u32x4 mk_srd(const void* p, u32 bytes) { const u64 a = (u64)p; return (u32x4){(u32)a, (u32)(a >> 32) & 0xffffu, bytes, 0x00020000u}; }
DI void dma1(u32 lds, u32 v0, u32x4 srd, u32 soff) {
  asm volatile("s_mov_b32 m0, %0\n\ts_nop 0\n\tbuffer_load_dwordx4 %1, %2, %3 offen lds" :: "s"(lds), "v"(v0), "s"(srd), "s"(soff) : "memory");
}
DI void dma2(u32 lds, u32 v0, u32x4 srd, u32 soff) {
  asm volatile("s_mov_b32 m0, %0\n\ts_nop 0\n\tbuffer_load_dwordx4 %1, %2, %3 offen lds\n\tbuffer_load_dwordx4 %1, %2, %3 offen offset:1024 lds"
               :: "s"(lds), "v"(v0), "s"(srd), "s"(soff) : "memory");
}
DI void dma4(u32 lds, u32 v0, u32 v1, u32 v2, u32 v3, u32x4 srd, u32 soff) {
  asm volatile("s_mov_b32 m0, %0\n\ts_nop 0\n\tbuffer_load_dwordx4 %1, %5, %6 offen lds\n\tbuffer_load_dwordx4 %2, %5, %6 offen offset:1024 lds\n\t"
               "buffer_load_dwordx4 %3, %5, %6 offen offset:2048 lds\n\tbuffer_load_dwordx4 %4, %5, %6 offen offset:3072 lds"
               :: "s"(lds), "v"(v0), "v"(v1), "v"(v2), "v"(v3), "s"(srd), "s"(soff) : "memory");
}
DI void setm0(u32 lds) { asm volatile("s_mov_b32 m0, %0\n\ts_nop 0" :: "s"(lds) : "memory"); }
template <int OFF> DI void dmao(u32 v, u32x4 srd, u32 soff) {
  asm volatile("buffer_load_dwordx4 %0, %1, %2 offen offset:%3 lds" :: "v"(v), "s"(srd), "s"(soff), "i"(OFF) : "memory");
}
#define PIN(x) asm volatile("" : "+v"(x))
DI u32 mk_tag(u32 e) { const u32 t = (e + 0x1B4E0193u) * 0x2F4A7C15u; return (t & 0x3FFFu) | ((t & 0x1FFFC000u) << 1) | ((t & 0x20000000u) << 2); }
DI u32 mk_tag2(u32 e) { return (e ^ 0x5BD1E995u) * 0x9E3779B1u; }
#define RSRC __amdgpu_buffer_rsrc_t
DI void st128c(RSRC r, u32 off, u32x4 v) { __builtin_amdgcn_raw_buffer_store_b128(v, r, off, 0, 16); }
DI void st64c(RSRC r, u32 off, u32x2 v) { __builtin_amdgcn_raw_buffer_store_b64(v, r, off, 0, 16); }
DI void st32c(RSRC r, u32 off, u32 v) { __builtin_amdgcn_raw_buffer_store_b32(v, r, off, 0, 16); }
DI u32x4 ld128c(RSRC r, u32 off) { return __builtin_amdgcn_raw_buffer_load_b128(r, off, 0, 16); }
DI u32x2 ld64c(RSRC r, u32 off) { return __builtin_amdgcn_raw_buffer_load_b64(r, off, 0, 16); }
DI bool flag_ok(u32x2 f, u32x2 tg) { return all_lanes(f.x == tg.x && f.y == tg.y); }
DI void wait_flags(RSRC r, u32 off, u32x2 tg) {
#pragma unroll 1
  for (u32 spins = 0; spins < SPIN_CAP; ++spins) {
    MEMB();
    if (flag_ok(ld64c(r, off), tg)) break;
    __builtin_amdgcn_s_sleep(PSLEEP);
  }
  MEMB();
}
DI void pace_wait(RSRC r, u32 off, u32 tg) {
#pragma unroll 1
  for (u32 spins = 0; spins < 12; ++spins) {
    MEMB();
    if (__builtin_amdgcn_readfirstlane(__builtin_amdgcn_raw_buffer_load_b32(r, off, 0, 16)) == tg) break;
    __builtin_amdgcn_s_sleep(PACE_SLEEP);
  }
  MEMB();
}
DI u32 tag_diff(u32 d, u32x4 v, u32 tag) { return d | (v.y ^ tag) | (v.w ^ tag); }
DI u32 uf_off(int blk, int w) { return WS_UF + (((blk & 7) * 4 + w) * 32 + (blk >> 3)) * 128; }

struct GB { u32 wr, rd; };
template <int TARGET> DI void gbar(const GB& g) {
  u32 t;
  asm volatile("v_mov_b32 %0, %3\n\ts_waitcnt lgkmcnt(0)\n\tds_write_b32 %1, %0\n"
               "1:\n\tds_read_b32 %0, %2\n\ts_waitcnt lgkmcnt(0)\n\tv_cmp_gt_u32 vcc, %3, %0\n\ts_cbranch_vccnz 1b"
               : "=&v"(t) : "v"(g.wr), "v"(g.rd), "i"(TARGET) : "memory", "vcc");
}

template <int TARGET> DI void gpost(const GB& g) {
  u32 t;
  asm volatile("v_mov_b32 %0, %2\n\tds_write_b32 %1, %0" : "=&v"(t) : "v"(g.wr), "i"(TARGET) : "memory");
}
template <int TARGET> DI void gcheck(const GB& g) {
  u32 t;
  asm volatile("1:\n\tds_read_b32 %0, %1\n\ts_waitcnt lgkmcnt(0)\n\tv_cmp_gt_u32 vcc, %2, %0\n\ts_cbranch_vccnz 1b"
               : "=&v"(t) : "v"(g.rd), "i"(TARGET) : "memory", "vcc");
}
template <int TARGET> DI void gverify(const GB& g, u32 pv) {
  asm volatile("v_cmp_gt_u32 vcc, %2, %0\n\ts_cbranch_vccz 2f\n"
               "1:\n\tds_read_b32 %0, %1\n\ts_waitcnt lgkmcnt(0)\n\tv_cmp_gt_u32 vcc, %2, %0\n\ts_cbranch_vccnz 1b\n2:"
               : "+v"(pv) : "v"(g.rd), "i"(TARGET) : "memory", "vcc");
}
template <int N> struct IC { static constexpr int v = N; };
template <int N> DI void waitv() { asm volatile("s_waitcnt vmcnt(%0)" :: "i"(N) : "memory"); }

DI float max3f(float a, float b, float c) { float r; asm("v_max3_f32 %0, %1, %2, %3" : "=v"(r) : "v"(a), "v"(b), "v"(c)); return r; }
template <class H> DI void softmax16h(const f32x16& p, u32x4& lo, u32x4& hi, H hook) {
  const float m0 = __builtin_fmaxf(p[0], p[1]);
  const float m1 = __builtin_fmaxf(p[14], p[15]);
  float ma = max3f(m0, p[2], p[3]), mb_ = max3f(m1, p[4], p[5]);
  ma = max3f(ma, p[6], p[7]); mb_ = max3f(mb_, p[8], p[9]);
  ma = max3f(ma, p[10], p[11]); mb_ = max3f(mb_, p[12], p[13]);
  const float L2E = 1.4426950408889634f;
  const float mb = max3f(ma, mb_, mb_) * -L2E;
  hook(IC<0>{});
  float e[16];
#pragma unroll
  for (int i = 0; i < 4; ++i) e[i] = __builtin_amdgcn_exp2f(__builtin_fmaf(p[i], L2E, mb));
  hook(IC<1>{});
#pragma unroll
  for (int i = 4; i < 8; ++i) e[i] = __builtin_amdgcn_exp2f(__builtin_fmaf(p[i], L2E, mb));
  hook(IC<2>{});
#pragma unroll
  for (int i = 8; i < 12; ++i) e[i] = __builtin_amdgcn_exp2f(__builtin_fmaf(p[i], L2E, mb));
  hook(IC<3>{});
#pragma unroll
  for (int i = 12; i < 16; ++i) e[i] = __builtin_amdgcn_exp2f(__builtin_fmaf(p[i], L2E, mb));
  hook(IC<4>{});
  const float s0 = ((e[0] + e[1]) + (e[2] + e[3])) + ((e[4] + e[5]) + (e[6] + e[7]));
  const float s1 = ((e[8] + e[9]) + (e[10] + e[11])) + ((e[12] + e[13]) + (e[14] + e[15]));
  const float r = __builtin_amdgcn_rcpf(s0 + s1);
  hook(IC<5>{});
  lo = (u32x4){pk2(e[0] * r, e[1] * r), pk2(e[2] * r, e[3] * r), pk2(e[4] * r, e[5] * r), pk2(e[6] * r, e[7] * r)};
  hook(IC<6>{});
  hi = (u32x4){pk2(e[8] * r, e[9] * r), pk2(e[10] * r, e[11] * r), pk2(e[12] * r, e[13] * r), pk2(e[14] * r, e[15] * r)};
  hook(IC<7>{});
}
DI void softmax16(const f32x16& p, u32x4& lo, u32x4& hi) { softmax16h(p, lo, hi, [](auto) NOPK {}); }
DI u32x4 pack8(const f32x16& a, int o) { return (u32x4){pk2(a[o], a[o + 1]), pk2(a[o + 2], a[o + 3]), pk2(a[o + 4], a[o + 5]), pk2(a[o + 6], a[o + 7])}; }

DI f32x2 cmul(f32x2 a, f32x2 b) { return (f32x2){__builtin_fmaf(-a.y, b.y, a.x * b.x), __builtin_fmaf(a.y, b.x, a.x * b.y)}; }
DI f32x2 cmulc(f32x2 a, f32x2 b) { return (f32x2){__builtin_fmaf(a.y, b.y, a.x * b.x), __builtin_fmaf(-a.x, b.y, a.y * b.x)}; }
DI f32x2 cexp_s(float a2, float brev, float s) {
  const float m = __builtin_amdgcn_exp2f(a2 * s);
  const float th = __builtin_amdgcn_fractf(brev * s);
  return (f32x2){m * __builtin_amdgcn_cosf(th), m * __builtin_amdgcn_sinf(th)};
}
template <int I> DI float mulb(float q, float x) {
  return __builtin_bit_cast(float, __builtin_amdgcn_update_dpp(0, __builtin_bit_cast(int, q), 0x150 + I, 0xf, 0xf, true)) * x;
}
template <int I> DI float fmab(float q, float x, float acc) {
  asm("v_fmac_f32_dpp %0, %1, %2 row_newbcast:%3 row_mask:0xf bank_mask:0xf" : "+v"(acc) : "v"(q), "v"(x), "i"(I));
  return acc;
}
template <int I> DI float fnmab(float q, float x, float acc) {
  asm("v_fmac_f32_dpp %0, -%1, %2 row_newbcast:%3 row_mask:0xf bank_mask:0xf" : "+v"(acc) : "v"(q), "v"(x), "i"(I));
  return acc;
}
template <int I> DI f32x2 cexp_b(float a2, float brev, float s) {
  const float m = __builtin_amdgcn_exp2f(mulb<I>(a2, s));
  const float th = __builtin_amdgcn_fractf(mulb<I>(brev, s));
  return (f32x2){m * __builtin_amdgcn_cosf(th), m * __builtin_amdgcn_sinf(th)};
}
template <int CTRL, int RM> DI float addd(float v) {
  return v + __builtin_bit_cast(float, __builtin_amdgcn_update_dpp(0, __builtin_bit_cast(int, v), CTRL, RM, 0xf, true));
}

template <int T0, int T1, int T2, int T3>
DI void conv_tiles(lptr ka, lptr ub, int ust, f32x4 (&acc)[4][4]) {
  constexpr int T[4] = {T0, T1, T2, T3};
  constexpr int NB = 2 * T3 + 2, PF = 8;
  u32x4 af[NB], bfr[4 * NB];
  bool have[NB] = {};
  auto lda = [&](int m) NOPK { af[m] = trd2(ka + m * 256, ka + m * 256 - 32); };
  auto ldb = [&](int n) NOPK {
    lptr q = ub + (n / NB) * ust + (n % NB) * 64;
    bfr[n] = lds128(q);
  };
  auto need = [&](int n) NOPK {
    ldb(n);
    if (n < NB) {
#pragma unroll
      for (int t = 0; t < 4; ++t) { const int m = 2 * T[t] + 1 - n; if (m >= 0 && !have[m]) { lda(m); have[m] = true; } }
    }
  };
  need(0); need(1);
#pragma unroll
  for (int n = 0; n < 4 * NB; ++n) {
    if (n < PF - 2) { need(2 * n + 2); need(2 * n + 3); }
    else if (n + PF < 4 * NB) need(n + PF);
    SCHEDB();
    const int rho = n / NB, be = n % NB;
#pragma unroll
    for (int t = 0; t < 4; ++t) {
      const int m = 2 * T[t] + 1 - be;
      if (m >= 0) acc[t][rho] = mfma16(af[m], bfr[n], be == 0 ? (f32x4){} : acc[t][rho]);
    }
    SCHEDB();
  }
}

#define PACE3 0
#define PFW 1
#define PROG 0
#define UPUB 1
#define ROT ((idx & 7) + 2 * (idx >> 3))
#define DBG_ARGS
#define DBG_PASS
constexpr int XR = 0, KR = 49152, VR = 61440, PHI = 77824, KVI = KR + 8192;
constexpr int CB = 0, CST = 8208;
constexpr int XCH = 0;
constexpr int YS = 16640;
constexpr int RAW = 26112;
constexpr int UT = 36352;
constexpr int UP = 45568, UST = 1168, UC2 = 9344;
constexpr int KL = 64256, KST = 4448;

struct AAddr { u32 xv0, xv1, xv2, xv3, wv0; };
DI AAddr a_addr(int p, int j, int w, int l) {
  AAddr a;
  a.xv0 = (128 * p + 32 * w + (l >> 3)) * 1024 + ((l & 7) ^ (l >> 4)) * 16;
  a.xv1 = (a.xv0 ^ 64) + 7168; a.xv2 = a.xv0 + 2 * 7168; a.xv3 = a.xv1 + 2 * 7168;
  a.wv0 = (l >> 2) * 3072 + j * 64 + (l & 3) * 16;
  return a;
}
DI void role_A(lptr sm, const int b, const char* x, const char* W, const char* wphi, const char* wo, char* out, char* ws DBG_ARGS) {
  const int xcd = b & 7, idx = b >> 3;
  const int p = (xcd >> 1) * 4 + (idx >> 3), j = (xcd & 1) * 8 + (idx & 7);
  const int tid = threadIdx.x;
  const int l = tid & 63, w = __builtin_amdgcn_readfirstlane(tid >> 6);
  const int hh = l >> 5, blk = (l >> 4) & 1, i16 = l & 15, g4 = l >> 4, q4 = i16 >> 2, pp = l & 3, r32 = l & 31;
  const u32 lb = (u32)(size_t)sm;
  const int r0 = ROT;
  const u32x4 sx = mk_srd(x, 2048 * 1024), sw = mk_srd(W, 512 * 3072);
  const AAddr ad = a_addr(p, j, w, l);
#pragma unroll
  for (int c = 0; c < 3; ++c) {
    dma4(lb + XR + c * 16384 + w * 4096, ad.xv0, ad.xv1, ad.xv2, ad.xv3, sx, ((c + r0) & 7) * 128);
    if (c < 2) dma1(lb + KR + c * 4096 + w * 1024, ad.wv0, sw, (((c + r0) & 7) * 64 + 16 * w) * 3072 + 1024);
  }
  dma1(lb + PHI, l * 16, mk_srd(wphi, 32 * 512), j * 1024);
  u32 epoch_;
  asm volatile("s_load_dword %0, %1, 0x0" : "=s"(epoch_) : "s"(ws) : "memory");
  const RSRC rs = __builtin_amdgcn_make_buffer_rsrc(ws, 0, WS_END, 0x00020000);
  const RSRC ro = __builtin_amdgcn_make_buffer_rsrc(out, 0, 4u << 20, 0x00020000);
  STAMPS_DECL; STAMP(1);
  const u32 wv1 = ad.wv0 + 48128, wv2 = ad.wv0 + 2 * 48128, wv3 = ad.wv0 + 3 * 48128;
  auto vslot = [&](int c) NOPK { return c < 7 ? VR + (c & 3) * 4096 : KR + 8192; };
  auto issueW = [&](int dst, int mat, int c) NOPK { dma1(lb + dst + w * 1024, ad.wv0, sw, (((c + r0) & 7) * 64 + 16 * w) * 3072 + mat * 1024); };
  const int rt = xcd * 4 + (idx >> 3), ct = idx & 7;
  const int sh = w & 1, kh = w >> 1, st = 2 * rt + sh;

  const int wlane = (8 * hh + q4) * 64 + blk * 32 + pp * 8;
  const int xsw = (r32 >> 1) & 7;
  u32 epoch;
  lptr vbase = sm + KR + wlane; PIN(vbase);
  u32x4 xf[32], phif[2];
  f32x16 ak = {}, av = {};
#pragma unroll
  for (int c = 0; c < 8; ++c) {
    if (c == 0) WAITV(10); else if (c < 6) WAITV(11); else WAITV(7);
    BAR();
    if (c == 0) { asm volatile("s_mov_b32 %0, %1" : "=s"(epoch) : "s"(epoch_)); STAMP(2); } if (c == 4) STAMP(3); if (c == 7) STAMP(4);
    lptr xs = sm + XR + (c % 3) * 16384 + w * 4096 + r32 * 128;
    lptr wsl = vbase + (c % 3) * 4096;
    lptr vsl = vbase + (vslot(c - 3) - KR);
    u32x4 wk[4], wv[4];
#pragma unroll
    for (int ks = 0; ks < 4; ++ks) { xf[c * 4 + ks] = lds128(xs + (((2 * ks + hh) ^ xsw) * 16)); wk[ks] = trd2(wsl + ks * 1024, wsl + ks * 1024 + 256); }
    if (c + 2 < 8) issueW(KR + ((c + 2) % 3) * 4096, 1, c + 2);
    if (c == 6) { issueW(vslot(6), 2, 6); issueW(vslot(7), 2, 7); }
    SCHEDB();
#pragma unroll
    for (int ks = 0; ks < 4; ++ks) {
      ak = mfma32(wk[ks], xf[c * 4 + ks], ak);
      if (c >= 3) wv[ks] = trd2(vsl + ks * 1024, vsl + ks * 1024 + 256);
      SCHEDB();
    }
    {
      const bool isx = c + 3 < 8, isq = c >= 6;
      const u32 so_ = isx ? (u32)(((c + 3 + r0) & 7) * 128) : (u32)(((4 * (c - 6) + w + r0) & 7) * 64 * 3072);
      if (isx || isq) setm0(lb + XR + ((isx ? c : c - 1) % 3) * 16384 + w * 4096);
      if (c >= 3) av = mfma32(xf[(c - 3) * 4], wv[0], av);
      if (isx) dmao<0>(ad.xv0, sx, so_); else if (isq) dmao<0>(ad.wv0, sw, so_);
      SCHEDB();
      if (c >= 3) av = mfma32(xf[(c - 3) * 4 + 1], wv[1], av);
      if (isx) dmao<1024>(ad.xv1, sx, so_); else if (isq) dmao<1024>(wv1, sw, so_);
      SCHEDB();
      if (c >= 3) av = mfma32(xf[(c - 3) * 4 + 2], wv[2], av);
      if (isx) dmao<2048>(ad.xv2, sx, so_); else if (isq) dmao<2048>(wv2, sw, so_);
      SCHEDB();
      if (c >= 3) av = mfma32(xf[(c - 3) * 4 + 3], wv[3], av);
      if (isx) dmao<3072>(ad.xv3, sx, so_); else if (isq) dmao<3072>(wv3, sw, so_);
      if (c < 6) issueW(vslot(c), 2, c);
    }
    if (c == PACE1C) st32c(rs, tid == 0 ? WS_PF + b * 128 : OOB, mk_tag(epoch));
    if (c == 2) {
      const u32 sg = ((l >> 2) & 1) ? 0x80008000u : 0u;
      lptr pb = sm + PHI + (4 * hh + q4) * 32 + (8 * blk + 4 * (pp >> 1)) * 2;
#pragma unroll
      for (int h = 0; h < 2; ++h) { u32x4 v = trd2(pb + h * 512, pb + h * 512 + 256); phif[h] = (u32x4){v.x ^ sg, v.y ^ sg, v.z ^ sg, v.w ^ sg}; }
    }
    SCHEDB();
  }
  STAMP(5);
  const u32 tag = mk_tag(epoch);
  const u32x2 ftag = {tag, mk_tag2(epoch)};
  const u32x4 so = mk_srd(wo, 512 * 1024);
  const u32 ov0 = ((l >> 2) & 7) * 1024 + ct * 128 + hh * 64 + pp * 16;
  const u32 ov1 = ov0 + 7168, ov2 = ov0 + 2 * 7168, ov3 = ov0 + 3 * 7168;
  constexpr int WOB[4] = {VR, 16384, 32768, 0};
  {
    constexpr int PT = XR + 16384, VT = KR;
    const f32x16 zero = {};
    f32x16 pk[2];
    pk[0] = mfma32(phif[0], pack8(ak, 0), zero); pk[1] = mfma32(phif[1], pack8(ak, 8), zero);
    lptr tb = sm + PT + w * 4096 + l * 32;
    {
      const char* t = l < 32 ? wo + (idx * 128 + w * 32 + l) * 128 : out + (size_t)(l < 48 ? 64 * rt : 64 * idx) * 2048;
      asm volatile("s_mov_b32 m0, %0\n\ts_nop 0\n\tglobal_load_lds_dword %1, off" :: "s"(lb + PHI + 1024), "v"(t) : "memory");
    }
    WAITV(9);
    BAR();
    STAMP(6);
    {
      u32x4 vf[12];
      auto ldv = [&](int i) NOPK { lptr q = vbase + (vslot(5 + (i >> 2)) - KR) + (i & 3) * 1024; vf[i] = trd2(q, q + 256); };
#pragma unroll
      for (int i = 0; i < 6; ++i) ldv(i);
      SCHEDB();
      u32x4 lo, hi;
      softmax16h(pk[0], lo, hi, [&](auto ii) NOPK {
        constexpr int i = decltype(ii)::v;
        SCHEDB();
        av = mfma32(xf[20 + i], vf[i], av);
        if constexpr (i + 6 < 12) ldv(i + 6);
        SCHEDB();
      });
      *(LAS u32x4*)tb = lo; *(LAS u32x4*)(tb + 16) = hi;
      softmax16h(pk[1], lo, hi, [&](auto ii) NOPK {
        constexpr int i = decltype(ii)::v;
        if constexpr (i < 2) { SCHEDB(); av = mfma32(xf[28 + 2 * i], vf[8 + 2 * i], av); av = mfma32(xf[29 + 2 * i], vf[9 + 2 * i], av); SCHEDB(); }
        if constexpr (i == 4) { SCHEDB(); *(LAS u32x4*)(sm + VT + w * 2048 + l * 16) = pack8(av, 0); SCHEDB(); }
        if constexpr (i == 5) { SCHEDB(); *(LAS u32x4*)(sm + VT + w * 2048 + 1024 + l * 16) = pack8(av, 8); SCHEDB(); }
        if constexpr (i == 6) { SCHEDB(); *(LAS u32x4*)(tb + 2048) = lo; SCHEDB(); }
      });
      *(LAS u32x4*)(tb + 2064) = hi;
    }
    STAMP(7);
    waitv<PFW>();
    BAR();
    STAMP(8);
    lptr ta = sm + PT + w * 1024 + (16 * hh + 4 * blk + q4) * 32 + pp * 8;
    lptr vb = sm + VT + hh * 1024 + (16 * (w >> 1) + i16 + 32 * blk) * 16;
    const u32 one = 0x3F803F80u;
    f32x4 kv = {}, kz = {};
#pragma unroll
    for (int s4 = 0; s4 < 4; ++s4) {
      const u32x4 a = trd2(ta + s4 * 4096, ta + s4 * 4096 + 256);
      kv = mfma16(a, lds128(vb + s4 * 2048), kv);
      kz = mfma16(a, (u32x4){one, one, one, one}, kz);
    }
    const u32 pbase = WS_KVP + (j * 16 + p) * KVS;
    st64c(rs, pbase + w * 512 + l * 8, (u32x2){pk2(kv[0], kv[1]), pk2(kv[2], kv[3])});
    st64c(rs, i16 == 0 ? pbase + 2048 + w * 32 + g4 * 8 : OOB, (u32x2){pk2(kz[0], kz[1]), pk2(kz[2], kz[3])});
    MEMB(); SCHEDB();
  }
  STAMP(9);
  f32x16 aq = {};
  {
    u32x4 wq[2][4];
    auto ldq = [&](int c) NOPK {
      lptr wsl = sm + XR + ((c < 4) ? 32768 : 0) + (c & 3) * 4096 + wlane;
#pragma unroll
      for (int ks = 0; ks < 4; ++ks) wq[c & 1][ks] = trd2(wsl + ks * 1024, wsl + ks * 1024 + 256);
    };
    ldq(0);
#pragma unroll
    for (int c = 0; c < 8; ++c) {
      if (c < 7) ldq(c + 1);
      SCHEDB();
#pragma unroll
      for (int ks = 0; ks < 4; ++ks) aq = mfma32(wq[c & 1][ks], xf[c * 4 + ks], aq);
      SCHEDB();
      if (c == KVFC) { WAITV(0); st64c(rs, l == 0 ? WS_KVF + ((j * 4 + w) * 16 + p) * 128 : OOB, ftag); STAMP(10); }
      if (c == 6) BAR();
    }
  }
  STAMP(11);
  auto issueO = [&](int i) NOPK { dma4(lb + WOB[i] + w * 4096, ov0, ov1, ov2, ov3, so, (128 * w + 32 * i) * 1024); };
  u32x4 pq[2][2], sel[2];
  const u32 kfo = WS_KVF + ((j * 4 + w) * 16 + i16) * 128;
  {
    const f32x16 zero = {};
    issueO(0);
    const f32x16 t0 = mfma32(phif[0], pack8(aq, 0), zero), t1 = mfma32(phif[1], pack8(aq, 8), zero);
    SCHEDB();
    issueO(1);
    const u32 onev = (l & 1) ? 0x3F800000u : 0x00003F80u;
#pragma unroll
    for (int cg = 0; cg < 2; ++cg)
#pragma unroll
      for (int d = 0; d < 4; ++d) sel[cg][d] = ((i16 >> 1) == 4 * cg + d) ? onev : 0u;
    PIN(sel[0]); PIN(sel[1]);
    issueO(2);
    softmax16(t0, pq[0][0], pq[0][1]); PIN(pq[0][0]); PIN(pq[0][1]);
    issueO(3);
    const u32x2 f0 = ld64c(rs, kfo);
    MEMB(); SCHEDB();
    softmax16(t1, pq[1][0], pq[1][1]); PIN(pq[1][0]); PIN(pq[1][1]);
    SCHEDB();
    STAMP(12);
    if (!flag_ok(f0, ftag)) wait_flags(rs, kfo, ftag);
    MEMB();
  }
  STAMP(13);
  {
    const u32 base = WS_KVP + j * 16 * KVS;
    u32x4 v[8];
#pragma unroll
    for (int i = 0; i < 8; ++i) v[i] = ld128c(rs, base + (4 * (i >> 1) + g4) * KVS + w * 512 + (i & 1) * 256 + i16 * 16);
    const u32x2 zv = ld64c(rs, base + (l >> 2) * KVS + 2048 + w * 32 + pp * 8);
    MEMB(); SCHEDB();
    f32x4 a = {};
#pragma unroll
    for (int i = 0; i < 8; ++i) a = mfma16(sel[i & 1], v[i], a);
    *(LAS u32x2*)(sm + KVI + w * 1024 + (pp >> 1) * 512 + (16 + (l >> 2)) * 16 + (pp & 1) * 8) = zv;
    *(LAS u32x2*)(sm + KVI + w * 1024 + hh * 512 + (2 * (l & 7) + blk) * 16 + (i16 >> 3) * 8) = (u32x2){pk2(a[0], a[1]), pk2(a[2], a[3])};
  }
  WAITV(0);
  BAR();
  STAMP(14);
  u32x4 wf[32];
  {
    const f32x16 zero = {};
    lptr ap = sm + KVI + hh * 1024 + r32 * 16;
    f32x16 t[2];
#pragma unroll
    for (int h = 0; h < 2; ++h) { t[h] = mfma32(lds128(ap + h * 2048), pq[h][0], zero); t[h] = mfma32(lds128(ap + h * 2048 + 512), pq[h][1], t[h]); }
#pragma unroll
    for (int h = 0; h < 2; ++h) {
      const float dh = ((t[h][8] + t[h][9]) + (t[h][10] + t[h][11])) + ((t[h][12] + t[h][13]) + (t[h][14] + t[h][15]));
      const auto sw2 = __builtin_amdgcn_permlane32_swap(f2u(dh), f2u(dh), false, false);
      const float r = __builtin_amdgcn_rcpf((u2f(sw2[0]) + u2f(sw2[1])) + 1e-6f);
      st128c(rs, WS_OFR + ((4 * p + w) * 32 + 2 * j + h) * 1024 + l * 16,
             (u32x4){pk2(t[h][0] * r, t[h][1] * r), pk2(t[h][2] * r, t[h][3] * r), pk2(t[h][4] * r, t[h][5] * r), pk2(t[h][6] * r, t[h][7] * r)});
    }
    MEMB(); SCHEDB();
    STAMP(15);
    lptr wb = sm + kh * 8192 + (4 * hh + q4) * 64 + blk * 32 + pp * 8;
    auto ldw = [&](int i) NOPK {
      lptr q = wb + WOB[(i >> 1) & 3] + (i >> 3) * 4096 + (i & 1) * 2048;
      wf[2 * i] = trd2(q, q + 1024); wf[2 * i + 1] = trd2(q + 512, q + 1536);
    };
#pragma unroll
    for (int i = 0; i < 16; ++i) ldw(i);
    WAITV(0);
    st64c(rs, l == 0 ? WS_OF + ((4 * p + w) * 16 + j) * 128 : OOB, ftag);
  }
  BAR();
  STAMP(16);
  if (OPRE) __builtin_amdgcn_s_sleep(OPRE);
  f32x16 acc0 = {}, acc1 = {};
  {
    const u32 obase = WS_OFR + (st * 32 + 16 * kh) * 1024 + l * 16;
    const u32 fo = WS_OF + (st * 16 + 8 * kh + (l & 7)) * 128;
    u32x4 of[16];
    wait_flags(rs, fo, ftag);
    STAMP(18);
#pragma unroll
    for (int i = 0; i < 16; ++i) of[i] = ld128c(rs, obase + i * 1024);
    MEMB(); SCHEDB();
#pragma unroll
    for (int i = 0; i < 16; ++i) { acc0 = mfma32(wf[2 * i], of[i], acc0); acc1 = mfma32(wf[2 * i + 1], of[i], acc1); }
  }
  MEMB(); SCHEDB();
  STAMP(19);
  {
    lptr rp = sm + kh * 17408 + (32 * sh + r32) * 272 + hh * 16;
#pragma unroll
    for (int g = 0; g < 4; ++g) *(LAS f32x4*)(rp + g * 32) = (f32x4){acc0[4 * g], acc0[4 * g + 1], acc0[4 * g + 2], acc0[4 * g + 3]};
#pragma unroll
    for (int g = 0; g < 4; ++g) *(LAS f32x4*)(rp + 128 + g * 32) = (f32x4){acc1[4 * g], acc1[4 * g + 1], acc1[4 * g + 2], acc1[4 * g + 3]};
    BAR();
#pragma unroll
    for (int i = 0; i < 2; ++i) {
      const int row = i * 32 + (tid >> 3), oc = tid & 7;
      lptr sp = sm + row * 272 + oc * 32;
      const f32x4 a = *(LAS f32x4*)sp + *(LAS f32x4*)(sp + 17408), bb = *(LAS f32x4*)(sp + 16) + *(LAS f32x4*)(sp + 17408 + 16);
      __builtin_amdgcn_raw_buffer_store_b128((u32x4){pk2(a[0], a[1]), pk2(a[2], a[3]), pk2(bb[0], bb[1]), pk2(bb[2], bb[3])}, ro, (64 * rt + row) * 2048 + ct * 128 + oc * 16, 0, OAUX);
    }
  }
  st32c(rs, (b == 0 && tid == 0) ? WS_EPOCH : OOB, epoch + 1);
  STAMP(20);
  STAMPS_OUT();
}

DI void prologue_B(u32 lb, const int b, const char* u, const char* Are, const char* Aim, const char* Bre, const char* Bim, const char* Cre, const char* Cim) {
  const int gam = b & 7, idx = b >> 3;
  const int l = threadIdx.x & 63, w = __builtin_amdgcn_readfirstlane(threadIdx.x >> 6);
  const int ar = l >> 3;
  const char* pa = ar < 2 ? (ar ? Aim : Are) : (ar < 4 ? ((ar & 1) ? Bim : Bre) : ((ar & 1) ? Cim : Cre));
  pa += (64 * gam + 2 * idx) * 256 + w * 128 + (l & 7) * 16;
  asm volatile("s_mov_b32 m0, %0\n\ts_nop 0\n\tglobal_load_lds_dwordx4 %1, off" :: "s"(lb + RAW + w * 1024), "v"(pa) : "memory");
  const u32 uo = (l >> 3) * 1024 + (((l & 7) ^ (2 * (l >> 5))) * 16);
  setm0(lb + UT + w * 2048);
  const u32x4 su = mk_srd(u, 2048 * 1024);
  const u32 so = (64 * idx + 16 * w) * 1024 + gam * 128;
  dmao<0>(uo, su, so); dmao<1024>((uo ^ 64) + 7168, su, so);
}

DI void role_B(lptr sm, const int b, const char* ldt, const char* Dsk, char* out, char* ws DBG_ARGS) {
  const int gam = b & 7, idx = b >> 3;
  const int tid = threadIdx.x;
  const int l = tid & 63, w = __builtin_amdgcn_readfirstlane(tid >> 6);
  const int g4 = l >> 4, i16 = l & 15, q4 = i16 >> 2, pp = l & 3, c = w >> 1, hs = w & 1;
  const RSRC rs = __builtin_amdgcn_make_buffer_rsrc(ws, 0, WS_END, 0x00020000);
  const RSRC ro = __builtin_amdgcn_make_buffer_rsrc(out, 0, 4u << 20, 0x00020000);
  STAMPS_DECL; STAMP(1);
  u32 epoch_, ld2_, dk2_, epa_;
  asm volatile("s_load_dword %0, %1, 0x0" : "=s"(epa_) : "s"(ws) : "memory");
  asm volatile("s_load_dword %0, %3, %4\n\ts_load_dword %1, %5, %7\n\ts_load_dword %2, %6, %7"
               : "=&s"(epoch_), "=&s"(ld2_), "=&s"(dk2_) : "s"(ws), "s"(WS_EPOCH + 128 * (1 + gam)), "s"(ldt), "s"(Dsk), "s"((64 * gam + 2 * idx) * 2) : "memory");
  lptr kb = sm + KL + c * KST + 40;
  lptr dummy = sm + KL + 2 * KST + l * 16;
  lptr zp = tid < 128 ? sm + UP + (tid >> 3) * UST + 16 + (tid & 7) * 16 : (tid < 136 ? sm + KL + ((tid >> 2) & 1) * KST + (tid & 3) * 16 : dummy);
  *(LAS u32x4*)zp = (u32x4){0, 0, 0, 0};
  const int beta = q4 == 0 ? pp : (q4 == 1 ? 256 + 2 * pp : (pp < 2 ? 256 * q4 + 4 * pp : 2 * q4 - 2 + pp));
  const bool first = (i16 == 0) && (hs == 0), lane0 = first && g4 == 0;
  lptr e1 = kb + (8 * i16 + 128 * hs + 256 * g4) * 2;
  lptr e2 = g4 == 0 ? dummy : e1 + 8;
  lptr e3 = g4 < 2 ? dummy : e1 - 508 * g4;
  lptr e4 = g4 < 2 ? (lane0 ? kb + 1022 * 2 : dummy) : ((first && g4 == 2) ? dummy : e1 - 4);
  lptr e5 = g4 == 1 ? e1 + 12 : ((first && g4 == 2) ? e1 - 2 : dummy);
  lptr e6 = (g4 == 1 && !first) ? e1 - 2 : dummy;
  lptr e7 = lane0 ? dummy : e1 + 2040;
  lptr e8 = lane0 ? kb + 1024 * 2 : ((l == 63 && hs == 1) ? kb + 2044 * 2 : dummy);
  PIN(e1); PIN(e2); PIN(e3); PIN(e4); PIN(e5); PIN(e6); PIN(e7); PIN(e8);
  u32 epoch, ld2, dk2;
  asm volatile("s_waitcnt vmcnt(0) lgkmcnt(0)\n\ts_barrier\n\ts_mov_b32 %0, %3\n\ts_mov_b32 %1, %4\n\ts_mov_b32 %2, %5"
               : "=&s"(epoch), "=&s"(ld2), "=&s"(dk2) : "s"(epoch_), "s"(ld2_), "s"(dk2_) : "memory");
  u32 epa; asm volatile("s_mov_b32 %0, %1" : "=s"(epa) : "s"(epa_));
  STAMP(2);
  const u32 tag = mk_tag(epoch);
  const u32x2 ftag = {tag, mk_tag2(epoch)};
  float a2, brev, ksum; f32x2 cc, m0, m1;
  u32x4 tv[2];
  {
#pragma unroll
    for (int e = 0; e < 2; ++e) {
      lptr a = sm + UT + (32 * (g4 >> 1) + 4 * q4 + 2 * e + (g4 & 1)) * 128 + (((2 * w + (pp >> 1)) ^ (2 * q4)) * 16) + (pp & 1) * 8;
      tv[e] = trd2(a, a + 16 * 128);
    }
    lptr rp = sm + RAW + w * 1024 + l * 2;
    const float are = bf1(*(LAS u16*)rp), aim = bf1(*(LAS u16*)(rp + 128)), bre = bf1(*(LAS u16*)(rp + 256)), bim = bf1(*(LAS u16*)(rp + 384));
    const float cre = bf1(*(LAS u16*)(rp + 512)), cim = bf1(*(LAS u16*)(rp + 640));
    float dt = rbf(__builtin_amdgcn_exp2f((c ? bf_hi(ld2) : bf_lo(ld2)) * 1.4426950408889634f));
    PIN(dt);
    SCHEDB();
#pragma unroll
    for (int e = 0; e < 2; ++e) *(LAS u32x4*)(sm + YS + (16 * w + i16) * 144 + (4 * e + 2 * (g4 & 1) + (g4 >> 1)) * 16) = tv[e];
#pragma unroll
    for (int e = 0; e < 2; ++e) tv[e] = lds128(sm + YS + (16 * w + 8 * e + (l >> 3)) * 144 + (l & 7) * 16);
    SCHEDB();
    cc = cmul((f32x2){cre, cim}, (f32x2){dt * bre, dt * bim});
    a2 = (dt * are) * 1.4426950408889634f; brev = (dt * aim) * 0.15915494309189535f;
    m0 = cexp_s(a2, brev, 128.f); m1 = cexp_s(a2, brev, 1024.f);
    PIN(a2); PIN(brev); PIN(cc); PIN(m0); PIN(m1);
    SCHEDB();
#pragma unroll
    for (int e = 0; e < 2; ++e) st128c(rs, WS_UX + (64 * gam + 16 * w + 8 * e + (l >> 3)) * 4096 + idx * 128 + (l & 7) * 16, tv[e]);
    const f32x2 g = cmul(cc, m1);
    ksum = __builtin_fmaf(-g.y, m1.y, g.x * m1.x);
    ksum = addd<0x111, 0xf>(ksum); ksum = addd<0x112, 0xf>(ksum); ksum = addd<0x114, 0xf>(ksum); ksum = addd<0x118, 0xf>(ksum);
    ksum = addd<0x142, 0xa>(ksum); ksum = addd<0x143, 0xc>(ksum);
    PIN(ksum);
  }
  MEMB(); SCHEDB();
  STAMP(3);
  {
    f32x4 dA = {}, dB = {}, dC = {}, dD = {};
    const float sA = (float)(8 * i16), sB = (float)beta;
    const u32 ufo = uf_off(gam + 8 * (8 * w + (l & 7)), idx >> 3);
    auto kgen = [&](auto kk) NOPK {
      constexpr int ks = decltype(kk)::v;
      u32x4 o1, o2, b1, b2;
      auto elem = [&](auto ee) NOPK {
        constexpr int e = decltype(ee)::v, I = 4 * ks + e;
        const f32x2 ea_ = cexp_b<I>(a2, brev, sA), eb = cexp_b<I>(a2, brev, sB);
        const f32x2 v1 = {fnmab<I>(cc.y, ea_.y, mulb<I>(cc.x, ea_.x)), fnmab<I>(cc.y, ea_.x, mulb<I>(cc.x, -ea_.y))};
        const f32x2 v2 = {fmab<I>(m0.y, v1.y, mulb<I>(m0.x, v1.x)), fnmab<I>(m0.y, v1.x, mulb<I>(m0.x, v1.y))};
        const f32x2 w2 = {fnmab<I>(m1.y, eb.y, mulb<I>(m1.x, eb.x)), fmab<I>(m1.y, eb.x, mulb<I>(m1.x, eb.y))};
        o1[e] = pk2(v1.x, v1.y); o2[e] = pk2(v2.x, v2.y); b1[e] = pk2(eb.x, eb.y); b2[e] = pk2(w2.x, w2.y);
      };
      elem(IC<0>{}); elem(IC<1>{}); elem(IC<2>{}); elem(IC<3>{});
      dA = mfma16(b1, o1, dA); dB = mfma16(b1, o2, dB); dC = mfma16(b2, o1, dC); dD = mfma16(b2, o2, dD);
      SCHEDB();
    };
    kgen(IC<0>{});
    if (UPUB == 1) kgen(IC<1>{});
    WAITV(0); st64c(rs, l == 0 ? uf_off(b, w) : OOB, ftag);
    STAMP(4);
    MEMB(); SCHEDB();
    if (UPUB == 0) kgen(IC<1>{});
    kgen(IC<2>{}); kgen(IC<3>{});
    STAMP(5);
    __builtin_amdgcn_s_setprio(BP1);
    lptr xc = sm + XCH + l * 16;
    *(LAS f32x4*)(xc + w * 1024) = hs ? dA : dB;
    *(LAS f32x2*)(xc + w * 1024 + 4096) = (f32x2){hs ? dC[0] : dD[0], ksum};
    BAR();
    f32x4 dt_ = (hs ? dB : dA) + *(LAS f32x4*)(xc + (w ^ 1) * 1024);
    const f32x2 dr = (f32x2){hs ? dD[0] : dC[0], ksum} + *(LAS f32x2*)(xc + (w ^ 1) * 1024 + 4096);
    const u32x2 f0 = ld64c(rs, ufo);
    STAMP(6);
    dt_[0] += lane0 ? (c ? bf_hi(dk2) : bf_lo(dk2)) : 0.f;
    {
      u32 p00 = pk2(dt_[0], dt_[0]), p01 = pk2(dt_[0], dt_[1]), p11 = pk2(dt_[1], dt_[1]), p12 = pk2(dt_[1], dt_[2]);
      u32 p22 = pk2(dt_[2], dt_[2]), p23 = pk2(dt_[2], dt_[3]);
      const float rp_ = __builtin_bit_cast(float, __builtin_amdgcn_update_dpp(0, __builtin_bit_cast(int, dr.x), 0xA0, 0xf, 0xf, true));
      u32 rr = pk2(dr.x, dr.x), rf = pk2(rp_, dr.x), kk = pk2(dr.y, dr.y);
      PIN(p00); PIN(p01); PIN(p11); PIN(p12); PIN(p22); PIN(p23); PIN(rr); PIN(kk);
      const u32 t2 = g4 == 1 ? p12 : p01, t8 = l == 0 ? rr : kk;
      *(LAS u32x4*)e7 = (u32x4){rf, rr, rr, rr};
      *(LAS u32x2*)e1 = (u32x2){g4 == 0 ? p01 : p00, g4 == 0 ? p23 : t2};
      *(LAS u32*)e2 = g4 == 1 ? p22 : p11;
      *(LAS u32*)e3 = p23;
      *(LAS u32*)e4 = g4 == 0 ? rr : p00;
      *(LAS u16*)e5 = (u16)(g4 == 1 ? p23 >> 16 : p00);
      *(LAS u16*)e6 = (u16)p00;
      *(LAS u32x2*)e8 = (u32x2){t8, t8};
    }
    MEMB(); SCHEDB();
    STAMP(7);
    if (!flag_ok(f0, ftag)) wait_flags(rs, ufo, ftag);
    MEMB();
    STAMP(8);
    u32x4 uv[2];
#pragma unroll
    for (int e = 0; e < 2; ++e) uv[e] = ld128c(rs, WS_UX + (64 * gam + 2 * idx + e) * 4096 + tid * 16);
#pragma unroll
    for (int e = 0; e < 2; ++e) {
      lptr q2 = sm + UP + (e * 4 + ((tid >> 1) & 3)) * UST + 128 + (tid >> 3) * 32 + (tid & 1) * 16;
      *(LAS u32x4*)q2 = uv[e];
      *(volatile LAS u32x2*)(q2 + UC2 + 8) = (u32x2){uv[e].x, uv[e].y};
      *(volatile LAS u32x2*)(q2 + UC2 + 16) = (u32x2){uv[e].z, uv[e].w};
    }
  }
  BAR();
  *(LAS f32x4*)(tid < 2 ? sm + CB + tid * CST : dummy) = (f32x4){};
  st32c(rs, (idx == 0 && tid == 0) ? WS_EPOCH + 128 * (1 + gam) : OOB, epoch + 1);
  STAMP(9);
  __builtin_amdgcn_s_setprio(BP1);
  if (PACE3) { pace_wait(rs, WS_PF + b * 128, mk_tag(epa)); if (PACE3 > 1) __builtin_amdgcn_s_sleep(PACE3); }
  if (PACE2) {
    const int p = (gam >> 1) * 4 + (idx >> 3), j = (gam & 1) * 8 + (idx & 7);
    pace_wait(rs, WS_KVF + ((j * 4 + w) * 16 + p) * 128, mk_tag(epa));
    if (PACE2 > 1) __builtin_amdgcn_s_sleep(PACE2);
  }
  {
    f32x4 acc[4][4];
    lptr ka = kb + (4 * pp - 4 * (8 * g4 + q4) + 112) * 2;
    lptr ub = sm + UP + c * 4 * UST + 128 + (4 * i16 + 8 * g4 - 60) * 2 + ((l & 1) ? 0 : UC2 + 8);
    if (hs == 0) conv_tiles<0, 3, 4, 7>(ka, ub, UST, acc); else conv_tiles<1, 2, 5, 6>(ka, ub, UST, acc);
    STAMP(10);
    __builtin_amdgcn_s_setprio(BP2);
    lptr cb = sm + CB + c * CST + 16;
    f32x4 y[4];
#pragma unroll
    for (int t = 0; t < 4; ++t) {
      const int tau = 2 * t + ((t & 1) ^ hs);
      const f32x4 *z = acc[t];
      *(LAS f32x4*)(cb + (64 * tau + l) * 16) = (f32x4){(z[1][3] + z[2][2]) + z[3][1], z[2][3] + z[3][2], z[3][3], z[3][3]};
    }
#pragma unroll
    for (int t = 0; t < 4; ++t) {
      const f32x4 *z = acc[t];
      y[t] = (f32x4){z[0][0], z[0][1] + z[1][0], (z[0][2] + z[1][1]) + z[2][0], (z[0][3] + z[1][2]) + (z[2][1] + z[3][0])};
      PIN(y[t]);
    }
    BAR();
    STAMP(11);
    const int src = g4 > 0 ? l - 16 : (i16 > 0 ? 47 + i16 : -1);
    u32x2 yp[4];
#pragma unroll
    for (int t = 0; t < 4; ++t) {
      const int tau = 2 * t + ((t & 1) ^ hs);
      const f32x4 r = *(LAS f32x4*)(cb + (64 * tau + src) * 16);
      yp[t] = (u32x2){pk2(y[t][0] + r[0], y[t][1] + r[1]), pk2(y[t][2] + r[2], y[t][3])};
    }
#pragma unroll
    for (int t = 0; t < 4; t += 2) {
      const auto s0 = __builtin_amdgcn_permlane16_swap(yp[t].x, yp[t + 1].x, false, false);
      const auto s1 = __builtin_amdgcn_permlane16_swap(yp[t].y, yp[t + 1].y, false, false);
      const int tau = (g4 & 1) ? 2 * t + 3 - hs : 2 * t + hs;
      st128c(rs, WS_YT + (64 * gam + 2 * idx + c) * 4096 + tau * 512 + q4 * 128 + (g4 >> 1) * 64 + pp * 16, (u32x4){s0[0], s1[0], s0[1], s1[1]});
    }
  }
  MEMB(); SCHEDB();
  STAMP(12);
  WAITV(0);
  st64c(rs, l == 0 ? WS_YF + ((gam * 4 + w) * 32 + idx) * 128 : OOB, ftag);
  STAMP(13);
  __builtin_amdgcn_s_sleep(YPRE);
  {
    const int tl = idx >> 2, hsn = ((tl >> 1) ^ tl) & 1;
    wait_flags(rs, WS_YF + ((gam * 4 + 2 * ((l >> 3) & 1) + hsn) * 32 + 8 * w + (l & 7)) * 128, ftag);
  }
  STAMP(14);
  {
    u32x4 v[2];
#pragma unroll
    for (int e = 0; e < 2; ++e) v[e] = ld128c(rs, WS_YT + (64 * gam + 16 * w + 8 * e + (l >> 3)) * 4096 + idx * 128 + (l & 7) * 16);
#pragma unroll
    for (int e = 0; e < 2; ++e) *(LAS u32x4*)(sm + YS + (16 * w + 8 * e + (l >> 3)) * 144 + (2 * pp + ((l >> 2) & 1)) * 16) = v[e];
  }
  BAR();
#pragma unroll
  for (int e = 0; e < 2; ++e) {
    lptr a = sm + YS + (8 * (4 * e + g4) + q4) * 144 + w * 32 + pp * 8;
    *(LAS u32x4*)(sm + UT + (16 * w + i16) * 144 + (4 * e + g4) * 16) = trd2(a, a + 4 * 144);
  }
#pragma unroll
  for (int e = 0; e < 2; ++e)
    __builtin_amdgcn_raw_buffer_store_b128(lds128(sm + UT + (16 * w + 8 * e + (l >> 3)) * 144 + (l & 7) * 16), ro, (64 * idx + 16 * w + 8 * e + (l >> 3)) * 2048 + 1024 + gam * 128 + (l & 7) * 16, 0, OAUX);
  STAMP(15);
  STAMPS_OUT();
}

#define RUN_A 1
#define RUN_B 1
__global__ __launch_bounds__(256, 2) NOPK void fused_kernel(const char* x, const char* W, const char* wphi, const char* wo, const char* u,
                                                            const char* Are, const char* Aim, const char* Bre, const char* Bim, const char* Cre, const char* Cim,
                                                            const char* ldt, const char* Dsk, char* out, char* ws) {
  extern __shared__ __attribute__((aligned(16))) char smem[];
  lptr sm = (lptr)smem;
  asm volatile("" :: "s"(x), "s"(W), "s"(wphi), "s"(wo), "s"(u), "s"(Are), "s"(Aim), "s"(Bre), "s"(Bim), "s"(Cre), "s"(Cim), "s"(ldt), "s"(Dsk), "s"(out), "s"(ws));
  const int b = blockIdx.x & 255;
  u32 isA = blockIdx.x >> 8;
  __builtin_amdgcn_s_setprio(3);
  if (!isA) prologue_B((u32)(size_t)sm, b, u, Are, Aim, Bre, Bim, Cre, Cim);
  asm volatile("" : "+s"(isA));
  if (__builtin_expect(isA, 1)) { __builtin_amdgcn_s_setprio(APRIO); role_A(sm, b, x, W, wphi, wo, out, ws DBG_PASS); }
  else { __builtin_amdgcn_s_setprio(BP0); role_B(sm, b, ldt, Dsk, out, ws DBG_PASS); }
}

extern "C" void kernel_launch(void* const* d_in, const int* in_sizes, int n_in, void* d_out, int out_size, void* d_ws, size_t ws_size,
                              hipStream_t stream) {
  (void)hipFuncSetAttribute((const void*)fused_kernel, hipFuncAttributeMaxDynamicSharedMemorySize, LDS_BYTES);
  fused_kernel<<<512, 256, LDS_BYTES, stream>>>((const char*)d_in[0], (const char*)d_in[1], (const char*)d_in[2], (const char*)d_in[3], (const char*)d_in[4],
                                                (const char*)d_in[5], (const char*)d_in[6], (const char*)d_in[7], (const char*)d_in[8], (const char*)d_in[9], (const char*)d_in[10],
                                                (const char*)d_in[11], (const char*)d_in[12], (char*)d_out, (char*)d_ws);
}
```

```cpp
#include <hip/hip_runtime.h>
#include <hip/hip_bf16.h>

typedef unsigned int u32;
typedef unsigned long long u64;
typedef unsigned short u16;
typedef short s16x4 __attribute__((ext_vector_type(4)));
typedef short s16x8 __attribute__((ext_vector_type(8)));
typedef __bf16 bf16x2 __attribute__((ext_vector_type(2)));
typedef __bf16 bf16x8 __attribute__((ext_vector_type(8)));
typedef float f32x2 __attribute__((ext_vector_type(2)));
typedef float f32x4 __attribute__((ext_vector_type(4)));
typedef float f32x16 __attribute__((ext_vector_type(16)));
typedef u32 u32x2 __attribute__((ext_vector_type(2)));
typedef u32 u32x4 __attribute__((ext_vector_type(4)));
#define LAS __attribute__((address_space(3)))
#define GAS __attribute__((address_space(1)))
#if defined(__HIP_DEVICE_COMPILE__)
#define NOPK __attribute__((target("no-packed-fp32-ops")))
#else
#define NOPK
#endif
#define DI __device__ __forceinline__ NOPK
typedef LAS char* lptr;

#define PSLEEP 16
#define OPRE 20
#define YPRE 20
#define UFK 1
#define APRIO 2
#define BP0 0
#define BP1 0
#define BP2 0
#define ADELAY 2
#define CONVT 0
#define KVFC 4
#define KSPLIT 3
#define OAUX 17
#define PACE1 0
#define PACE2 0
#define PACE_SLEEP 2
#define PACE1C 7
#define SWAP_ROLES 1
constexpr u32 WS_EPOCH = 0;
constexpr u32 WS_OF = 4096;
constexpr u32 WS_UF = WS_OF + 1024 * 128;
constexpr u32 WS_YF = WS_UF + 1024 * 128;
constexpr u32 WS_KVF = WS_YF + 1024 * 128;
constexpr u32 WS_PF = WS_KVF + 1024 * 128;
constexpr u32 WS_KVP = 1u << 20;
constexpr u32 KVS = 2176;
constexpr u32 WS_OFR = 2u << 20;
constexpr u32 WS_UX = 4u << 20;
constexpr u32 WS_YT = 6u << 20;
constexpr u32 WS_END = 8u << 20;
constexpr u32 OOB = 0x7FFFFFF0u;
constexpr int LDS_BYTES = 81920;
constexpr u32 SPIN_CAP = 1u << 16;

#define STAMPS_DECL u64 probe_ = tbeg_
#define STAMP(k) do { if ((k) == PROBE_SEL) { asm volatile("s_memrealtime %0\n\ts_waitcnt lgkmcnt(0)" : "=s"(probe_) :: "memory"); } } while (0)
#define STAMPS_OUT() do { while ((long long)(__builtin_amdgcn_s_memrealtime() - probe_) < 3000) __builtin_amdgcn_s_sleep(4); } while (0)

#define BAR() asm volatile("s_waitcnt lgkmcnt(0)\n\ts_barrier" ::: "memory")
#define WAITV(n) asm volatile("s_waitcnt vmcnt(" #n ")" ::: "memory")
#define MEMB() asm volatile("" ::: "memory")
#define SCHEDB() __builtin_amdgcn_sched_barrier(0)

DI float u2f(u32 v) { return __builtin_bit_cast(float, v); }
DI u32 f2u(float v) { return __builtin_bit_cast(u32, v); }
DI bool all_lanes(bool c) { return __builtin_amdgcn_ballot_w64(!c) == 0; }
DI u32 pk2(float a, float b) { f32x2 v = {a, b}; return __builtin_bit_cast(u32, __builtin_convertvector(v, bf16x2)); }
DI float bf_lo(u32 v) { return u2f(v << 16); }
DI float bf_hi(u32 v) { return u2f(v & 0xffff0000u); }
DI float bf1(u16 v) { return u2f((u32)v << 16); }
DI float rbf(float a) { return bf_lo(pk2(a, a)); }
DI u32x2 trd(lptr p) { return __builtin_bit_cast(u32x2, __builtin_amdgcn_ds_read_tr16_b64_v4i16((LAS s16x4*)p)); }
DI u32x4 trd2(lptr p0, lptr p1) { u32x2 a = trd(p0), b = trd(p1); return (u32x4){a.x, a.y, b.x, b.y}; }
DI u32x4 lds128(lptr p) { return *(LAS u32x4*)p; }
DI u32x2 lds64(lptr p) { return *(volatile LAS u32x2*)p; }
DI f32x16 mfma32(u32x4 a, u32x4 b, f32x16 c) { return __builtin_amdgcn_mfma_f32_32x32x16_bf16(__builtin_bit_cast(bf16x8, a), __builtin_bit_cast(bf16x8, b), c, 0, 0, 0); }
DI f32x4 mfma16(u32x4 a, u32x4 b, f32x4 c) { return __builtin_amdgcn_mfma_f32_16x16x32_bf16(__builtin_bit_cast(bf16x8, a), __builtin_bit_cast(bf16x8, b), c, 0, 0, 0); }
DI u32x4 mk_srd(const void* p, u32 bytes) { const u64 a = (u64)p; return (u32x4){(u32)a, (u32)(a >> 32) & 0xffffu, bytes, 0x00020000u}; }
DI void dma1(u32 lds, u32 v0, u32x4 srd, u32 soff) {
  asm volatile("s_mov_b32 m0, %0\n\ts_nop 0\n\tbuffer_load_dwordx4 %1, %2, %3 offen lds" :: "s"(lds), "v"(v0), "s"(srd), "s"(soff) : "memory");
}
DI void dma2(u32 lds, u32 v0, u32x4 srd, u32 soff) {
  asm volatile("s_mov_b32 m0, %0\n\ts_nop 0\n\tbuffer_load_dwordx4 %1, %2, %3 offen lds\n\tbuffer_load_dwordx4 %1, %2, %3 offen offset:1024 lds"
               :: "s"(lds), "v"(v0), "s"(srd), "s"(soff) : "memory");
}
DI void dma4(u32 lds, u32 v0, u32 v1, u32 v2, u32 v3, u32x4 srd, u32 soff) {
  asm volatile("s_mov_b32 m0, %0\n\ts_nop 0\n\tbuffer_load_dwordx4 %1, %5, %6 offen lds\n\tbuffer_load_dwordx4 %2, %5, %6 offen offset:1024 lds\n\t"
               "buffer_load_dwordx4 %3, %5, %6 offen offset:2048 lds\n\tbuffer_load_dwordx4 %4, %5, %6 offen offset:3072 lds"
               :: "s"(lds), "v"(v0), "v"(v1), "v"(v2), "v"(v3), "s"(srd), "s"(soff) : "memory");
}
DI void setm0(u32 lds) { asm volatile("s_mov_b32 m0, %0\n\ts_nop 0" :: "s"(lds) : "memory"); }
template <int OFF> DI void dmao(u32 v, u32x4 srd, u32 soff) {
  asm volatile("buffer_load_dwordx4 %0, %1, %2 offen offset:%3 lds" :: "v"(v), "s"(srd), "s"(soff), "i"(OFF) : "memory");
}
#define PIN(x) asm volatile("" : "+v"(x))
DI u32 mk_tag(u32 e) { const u32 t = (e + 0x1B4E0193u) * 0x2F4A7C15u; return (t & 0x3FFFu) | ((t & 0x1FFFC000u) << 1) | ((t & 0x20000000u) << 2); }
DI u32 mk_tag2(u32 e) { return (e ^ 0x5BD1E995u) * 0x9E3779B1u; }
#define RSRC __amdgpu_buffer_rsrc_t
DI void st128c(RSRC r, u32 off, u32x4 v) { __builtin_amdgcn_raw_buffer_store_b128(v, r, off, 0, 16); }
DI void st64c(RSRC r, u32 off, u32x2 v) { __builtin_amdgcn_raw_buffer_store_b64(v, r, off, 0, 16); }
DI void st32c(RSRC r, u32 off, u32 v) { __builtin_amdgcn_raw_buffer_store_b32(v, r, off, 0, 16); }
DI u32x4 ld128c(RSRC r, u32 off) { return __builtin_amdgcn_raw_buffer_load_b128(r, off, 0, 16); }
DI u32x2 ld64c(RSRC r, u32 off) { return __builtin_amdgcn_raw_buffer_load_b64(r, off, 0, 16); }
DI bool flag_ok(u32x2 f, u32x2 tg) { return all_lanes(f.x == tg.x && f.y == tg.y); }
DI void wait_flags(RSRC r, u32 off, u32x2 tg) {
#pragma unroll 1
  for (u32 spins = 0; spins < SPIN_CAP; ++spins) {
    MEMB();
    if (flag_ok(ld64c(r, off), tg)) break;
    __builtin_amdgcn_s_sleep(PSLEEP);
  }
  MEMB();
}
DI void pace_wait(RSRC r, u32 off, u32 tg) {
#pragma unroll 1
  for (u32 spins = 0; spins < 12; ++spins) {
    MEMB();
    if (__builtin_amdgcn_readfirstlane(__builtin_amdgcn_raw_buffer_load_b32(r, off, 0, 16)) == tg) break;
    __builtin_amdgcn_s_sleep(PACE_SLEEP);
  }
  MEMB();
}
DI u32 tag_diff(u32 d, u32x4 v, u32 tag) { return d | (v.y ^ tag) | (v.w ^ tag); }
DI u32 uf_off(int blk, int w) { return WS_UF + (((blk & 7) * 4 + w) * 32 + (blk >> 3)) * 128; }

struct GB { u32 wr, rd; };
template <int TARGET> DI void gbar(const GB& g) {
  u32 t;
  asm volatile("v_mov_b32 %0, %3\n\ts_waitcnt lgkmcnt(0)\n\tds_write_b32 %1, %0\n"
               "1:\n\tds_read_b32 %0, %2\n\ts_waitcnt lgkmcnt(0)\n\tv_cmp_gt_u32 vcc, %3, %0\n\ts_cbranch_vccnz 1b"
               : "=&v"(t) : "v"(g.wr), "v"(g.rd), "i"(TARGET) : "memory", "vcc");
}

template <int TARGET> DI void gpost(const GB& g) {
  u32 t;
  asm volatile("v_mov_b32 %0, %2\n\tds_write_b32 %1, %0" : "=&v"(t) : "v"(g.wr), "i"(TARGET) : "memory");
}
template <int TARGET> DI void gcheck(const GB& g) {
  u32 t;
  asm volatile("1:\n\tds_read_b32 %0, %1\n\ts_waitcnt lgkmcnt(0)\n\tv_cmp_gt_u32 vcc, %2, %0\n\ts_cbranch_vccnz 1b"
               : "=&v"(t) : "v"(g.rd), "i"(TARGET) : "memory", "vcc");
}
template <int TARGET> DI void gverify(const GB& g, u32 pv) {
  asm volatile("v_cmp_gt_u32 vcc, %2, %0\n\ts_cbranch_vccz 2f\n"
               "1:\n\tds_read_b32 %0, %1\n\ts_waitcnt lgkmcnt(0)\n\tv_cmp_gt_u32 vcc, %2, %0\n\ts_cbranch_vccnz 1b\n2:"
               : "+v"(pv) : "v"(g.rd), "i"(TARGET) : "memory", "vcc");
}
template <int N> struct IC { static constexpr int v = N; };
template <int N> DI void waitv() { asm volatile("s_waitcnt vmcnt(%0)" :: "i"(N) : "memory"); }

DI float max3f(float a, float b, float c) { float r; asm("v_max3_f32 %0, %1, %2, %3" : "=v"(r) : "v"(a), "v"(b), "v"(c)); return r; }
template <class H> DI void softmax16h(const f32x16& p, u32x4& lo, u32x4& hi, H hook) {
  const float m0 = __builtin_fmaxf(p[0], p[1]);
  const float m1 = __builtin_fmaxf(p[14], p[15]);
  float ma = max3f(m0, p[2], p[3]), mb_ = max3f(m1, p[4], p[5]);
  ma = max3f(ma, p[6], p[7]); mb_ = max3f(mb_, p[8], p[9]);
  ma = max3f(ma, p[10], p[11]); mb_ = max3f(mb_, p[12], p[13]);
  const float L2E = 1.4426950408889634f;
  const float mb = max3f(ma, mb_, mb_) * -L2E;
  hook(IC<0>{});
  float e[16];
#pragma unroll
  for (int i = 0; i < 4; ++i) e[i] = __builtin_amdgcn_exp2f(__builtin_fmaf(p[i], L2E, mb));
  hook(IC<1>{});
#pragma unroll
  for (int i = 4; i < 8; ++i) e[i] = __builtin_amdgcn_exp2f(__builtin_fmaf(p[i], L2E, mb));
  hook(IC<2>{});
#pragma unroll
  for (int i = 8; i < 12; ++i) e[i] = __builtin_amdgcn_exp2f(__builtin_fmaf(p[i], L2E, mb));
  hook(IC<3>{});
#pragma unroll
  for (int i = 12; i < 16; ++i) e[i] = __builtin_amdgcn_exp2f(__builtin_fmaf(p[i], L2E, mb));
  hook(IC<4>{});
  const float s0 = ((e[0] + e[1]) + (e[2] + e[3])) + ((e[4] + e[5]) + (e[6] + e[7]));
  const float s1 = ((e[8] + e[9]) + (e[10] + e[11])) + ((e[12] + e[13]) + (e[14] + e[15]));
  const float r = __builtin_amdgcn_rcpf(s0 + s1);
  hook(IC<5>{});
  lo = (u32x4){pk2(e[0] * r, e[1] * r), pk2(e[2] * r, e[3] * r), pk2(e[4] * r, e[5] * r), pk2(e[6] * r, e[7] * r)};
  hook(IC<6>{});
  hi = (u32x4){pk2(e[8] * r, e[9] * r), pk2(e[10] * r, e[11] * r), pk2(e[12] * r, e[13] * r), pk2(e[14] * r, e[15] * r)};
  hook(IC<7>{});
}
DI void softmax16(const f32x16& p, u32x4& lo, u32x4& hi) { softmax16h(p, lo, hi, [](auto) NOPK {}); }
DI u32x4 pack8(const f32x16& a, int o) { return (u32x4){pk2(a[o], a[o + 1]), pk2(a[o + 2], a[o + 3]), pk2(a[o + 4], a[o + 5]), pk2(a[o + 6], a[o + 7])}; }

DI f32x2 cmul(f32x2 a, f32x2 b) { return (f32x2){__builtin_fmaf(-a.y, b.y, a.x * b.x), __builtin_fmaf(a.y, b.x, a.x * b.y)}; }
DI f32x2 cmulc(f32x2 a, f32x2 b) { return (f32x2){__builtin_fmaf(a.y, b.y, a.x * b.x), __builtin_fmaf(-a.x, b.y, a.y * b.x)}; }
DI f32x2 cexp_s(float a2, float brev, float s) {
  const float m = __builtin_amdgcn_exp2f(a2 * s);
  const float th = __builtin_amdgcn_fractf(brev * s);
  return (f32x2){m * __builtin_amdgcn_cosf(th), m * __builtin_amdgcn_sinf(th)};
}
template <int I> DI float mulb(float q, float x) {
  return __builtin_bit_cast(float, __builtin_amdgcn_update_dpp(0, __builtin_bit_cast(int, q), 0x150 + I, 0xf, 0xf, true)) * x;
}
template <int I> DI float fmab(float q, float x, float acc) {
  asm("v_fmac_f32_dpp %0, %1, %2 row_newbcast:%3 row_mask:0xf bank_mask:0xf" : "+v"(acc) : "v"(q), "v"(x), "i"(I));
  return acc;
}
template <int I> DI float fnmab(float q, float x, float acc) {
  asm("v_fmac_f32_dpp %0, -%1, %2 row_newbcast:%3 row_mask:0xf bank_mask:0xf" : "+v"(acc) : "v"(q), "v"(x), "i"(I));
  return acc;
}
template <int I> DI f32x2 cexp_b(float a2, float brev, float s) {
  const float m = __builtin_amdgcn_exp2f(mulb<I>(a2, s));
  const float th = __builtin_amdgcn_fractf(mulb<I>(brev, s));
  return (f32x2){m * __builtin_amdgcn_cosf(th), m * __builtin_amdgcn_sinf(th)};
}
template <int CTRL, int RM> DI float addd(float v) {
  return v + __builtin_bit_cast(float, __builtin_amdgcn_update_dpp(0, __builtin_bit_cast(int, v), CTRL, RM, 0xf, true));
}

template <int T0, int T1, int T2, int T3>
DI void conv_tiles(lptr ka, lptr ub, int ust, f32x4 (&acc)[4][4]) {
  constexpr int T[4] = {T0, T1, T2, T3};
  constexpr int NB = 2 * T3 + 2, PF = 8;
  u32x4 af[NB], bfr[4 * NB];
  bool have[NB] = {};
  auto lda = [&](int m) NOPK { af[m] = trd2(ka + m * 256, ka + m * 256 - 32); };
  auto ldb = [&](int n) NOPK {
    lptr q = ub + (n / NB) * ust + (n % NB) * 64;
    bfr[n] = lds128(q);
  };
  auto need = [&](int n) NOPK {
    ldb(n);
    if (n < NB) {
#pragma unroll
      for (int t = 0; t < 4; ++t) { const int m = 2 * T[t] + 1 - n; if (m >= 0 && !have[m]) { lda(m); have[m] = true; } }
    }
  };
  need(0); need(1);
#pragma unroll
  for (int n = 0; n < 4 * NB; ++n) {
    if (n < PF - 2) { need(2 * n + 2); need(2 * n + 3); }
    else if (n + PF < 4 * NB) need(n + PF);
    SCHEDB();
    const int rho = n / NB, be = n % NB;
#pragma unroll
    for (int t = 0; t < 4; ++t) {
      const int m = 2 * T[t] + 1 - be;
      if (m >= 0) acc[t][rho] = mfma16(af[m], bfr[n], be == 0 ? (f32x4){} : acc[t][rho]);
    }
    SCHEDB();
  }
}

#define PACE3 0
#define PFW 1
#define PROG 0
#define UPUB 1
#define ROT ((idx & 7) + 2 * (idx >> 3))
#define DBG_ARGS , u64 tbeg_
#define DBG_PASS , tbeg_
constexpr int XR = 0, KR = 49152, VR = 61440, PHI = 77824, KVI = KR + 8192;
constexpr int CB = 0, CST = 8208;
constexpr int XCH = 0;
constexpr int YS = 16640;
constexpr int RAW = 26112;
constexpr int UT = 36352;
constexpr int UP = 45568, UST = 1168, UC2 = 9344;
constexpr int KL = 64256, KST = 4448;

struct AAddr { u32 xv0, xv1, xv2, xv3, wv0; };
DI AAddr a_addr(int p, int j, int w, int l) {
  AAddr a;
  a.xv0 = (128 * p + 32 * w + (l >> 3)) * 1024 + ((l & 7) ^ (l >> 4)) * 16;
  a.xv1 = (a.xv0 ^ 64) + 7168; a.xv2 = a.xv0 + 2 * 7168; a.xv3 = a.xv1 + 2 * 7168;
  a.wv0 = (l >> 2) * 3072 + j * 64 + (l & 3) * 16;
  return a;
}
#define PROBE_SEL 99
DI void role_A(lptr sm, const int b, const char* x, const char* W, const char* wphi, const char* wo, char* out, char* ws DBG_ARGS) {
  const int xcd = b & 7, idx = b >> 3;
  const int p = (xcd >> 1) * 4 + (idx >> 3), j = (xcd & 1) * 8 + (idx & 7);
  const int tid = threadIdx.x;
  const int l = tid & 63, w = __builtin_amdgcn_readfirstlane(tid >> 6);
  const int hh = l >> 5, blk = (l >> 4) & 1, i16 = l & 15, g4 = l >> 4, q4 = i16 >> 2, pp = l & 3, r32 = l & 31;
  const u32 lb = (u32)(size_t)sm;
  const int r0 = ROT;
  const u32x4 sx = mk_srd(x, 2048 * 1024), sw = mk_srd(W, 512 * 3072);
  const AAddr ad = a_addr(p, j, w, l);
#pragma unroll
  for (int c = 0; c < 3; ++c) {
    dma4(lb + XR + c * 16384 + w * 4096, ad.xv0, ad.xv1, ad.xv2, ad.xv3, sx, ((c + r0) & 7) * 128);
    if (c < 2) dma1(lb + KR + c * 4096 + w * 1024, ad.wv0, sw, (((c + r0) & 7) * 64 + 16 * w) * 3072 + 1024);
  }
  dma1(lb + PHI, l * 16, mk_srd(wphi, 32 * 512), j * 1024);
  u32 epoch_;
  asm volatile("s_load_dword %0, %1, 0x0" : "=s"(epoch_) : "s"(ws) : "memory");
  const RSRC rs = __builtin_amdgcn_make_buffer_rsrc(ws, 0, WS_END, 0x00020000);
  const RSRC ro = __builtin_amdgcn_make_buffer_rsrc(out, 0, 4u << 20, 0x00020000);
  STAMPS_DECL; STAMP(1);
  const u32 wv1 = ad.wv0 + 48128, wv2 = ad.wv0 + 2 * 48128, wv3 = ad.wv0 + 3 * 48128;
  auto vslot = [&](int c) NOPK { return c < 7 ? VR + (c & 3) * 4096 : KR + 8192; };
  auto issueW = [&](int dst, int mat, int c) NOPK { dma1(lb + dst + w * 1024, ad.wv0, sw, (((c + r0) & 7) * 64 + 16 * w) * 3072 + mat * 1024); };
  const int rt = xcd * 4 + (idx >> 3), ct = idx & 7;
  const int sh = w & 1, kh = w >> 1, st = 2 * rt + sh;

  const int wlane = (8 * hh + q4) * 64 + blk * 32 + pp * 8;
  const int xsw = (r32 >> 1) & 7;
  u32 epoch;
  lptr vbase = sm + KR + wlane; PIN(vbase);
  u32x4 xf[32], phif[2];
  f32x16 ak = {}, av = {};
#pragma unroll
  for (int c = 0; c < 8; ++c) {
    if (c == 0) WAITV(10); else if (c < 6) WAITV(11); else WAITV(7);
    BAR();
    if (c == 0) { asm volatile("s_mov_b32 %0, %1" : "=s"(epoch) : "s"(epoch_)); STAMP(2); } if (c == 4) STAMP(3); if (c == 7) STAMP(4);
    lptr xs = sm + XR + (c % 3) * 16384 + w * 4096 + r32 * 128;
    lptr wsl = vbase + (c % 3) * 4096;
    lptr vsl = vbase + (vslot(c - 3) - KR);
    u32x4 wk[4], wv[4];
#pragma unroll
    for (int ks = 0; ks < 4; ++ks) { xf[c * 4 + ks] = lds128(xs + (((2 * ks + hh) ^ xsw) * 16)); wk[ks] = trd2(wsl + ks * 1024, wsl + ks * 1024 + 256); }
    if (c + 2 < 8) issueW(KR + ((c + 2) % 3) * 4096, 1, c + 2);
    if (c == 6) { issueW(vslot(6), 2, 6); issueW(vslot(7), 2, 7); }
    SCHEDB();
#pragma unroll
    for (int ks = 0; ks < 4; ++ks) {
      ak = mfma32(wk[ks], xf[c * 4 + ks], ak);
      if (c >= 3) wv[ks] = trd2(vsl + ks * 1024, vsl + ks * 1024 + 256);
      SCHEDB();
    }
    {
      const bool isx = c + 3 < 8, isq = c >= 6;
      const u32 so_ = isx ? (u32)(((c + 3 + r0) & 7) * 128) : (u32)(((4 * (c - 6) + w + r0) & 7) * 64 * 3072);
      if (isx || isq) setm0(lb + XR + ((isx ? c : c - 1) % 3) * 16384 + w * 4096);
      if (c >= 3) av = mfma32(xf[(c - 3) * 4], wv[0], av);
      if (isx) dmao<0>(ad.xv0, sx, so_); else if (isq) dmao<0>(ad.wv0, sw, so_);
      SCHEDB();
      if (c >= 3) av = mfma32(xf[(c - 3) * 4 + 1], wv[1], av);
      if (isx) dmao<1024>(ad.xv1, sx, so_); else if (isq) dmao<1024>(wv1, sw, so_);
      SCHEDB();
      if (c >= 3) av = mfma32(xf[(c - 3) * 4 + 2], wv[2], av);
      if (isx) dmao<2048>(ad.xv2, sx, so_); else if (isq) dmao<2048>(wv2, sw, so_);
      SCHEDB();
      if (c >= 3) av = mfma32(xf[(c - 3) * 4 + 3], wv[3], av);
      if (isx) dmao<3072>(ad.xv3, sx, so_); else if (isq) dmao<3072>(wv3, sw, so_);
      if (c < 6) issueW(vslot(c), 2, c);
    }
    if (c == PACE1C) st32c(rs, tid == 0 ? WS_PF + b * 128 : OOB, mk_tag(epoch));
    if (c == 2) {
      const u32 sg = ((l >> 2) & 1) ? 0x80008000u : 0u;
      lptr pb = sm + PHI + (4 * hh + q4) * 32 + (8 * blk + 4 * (pp >> 1)) * 2;
#pragma unroll
      for (int h = 0; h < 2; ++h) { u32x4 v = trd2(pb + h * 512, pb + h * 512 + 256); phif[h] = (u32x4){v.x ^ sg, v.y ^ sg, v.z ^ sg, v.w ^ sg}; }
    }
    SCHEDB();
  }
  STAMP(5);
  const u32 tag = mk_tag(epoch);
  const u32x2 ftag = {tag, mk_tag2(epoch)};
  const u32x4 so = mk_srd(wo, 512 * 1024);
  const u32 ov0 = ((l >> 2) & 7) * 1024 + ct * 128 + hh * 64 + pp * 16;
  const u32 ov1 = ov0 + 7168, ov2 = ov0 + 2 * 7168, ov3 = ov0 + 3 * 7168;
  constexpr int WOB[4] = {VR, 16384, 32768, 0};
  {
    constexpr int PT = XR + 16384, VT = KR;
    const f32x16 zero = {};
    f32x16 pk[2];
    pk[0] = mfma32(phif[0], pack8(ak, 0), zero); pk[1] = mfma32(phif[1], pack8(ak, 8), zero);
    lptr tb = sm + PT + w * 4096 + l * 32;
    {
      const char* t = l < 32 ? wo + (idx * 128 + w * 32 + l) * 128 : out + (size_t)(l < 48 ? 64 * rt : 64 * idx) * 2048;
      asm volatile("s_mov_b32 m0, %0\n\ts_nop 0\n\tglobal_load_lds_dword %1, off" :: "s"(lb + PHI + 1024), "v"(t) : "memory");
    }
    WAITV(9);
    BAR();
    STAMP(6);
    {
      u32x4 vf[12];
      auto ldv = [&](int i) NOPK { lptr q = vbase + (vslot(5 + (i >> 2)) - KR) + (i & 3) * 1024; vf[i] = trd2(q, q + 256); };
#pragma unroll
      for (int i = 0; i < 6; ++i) ldv(i);
      SCHEDB();
      u32x4 lo, hi;
      softmax16h(pk[0], lo, hi, [&](auto ii) NOPK {
        constexpr int i = decltype(ii)::v;
        SCHEDB();
        av = mfma32(xf[20 + i], vf[i], av);
        if constexpr (i + 6 < 12) ldv(i + 6);
        SCHEDB();
      });
      *(LAS u32x4*)tb = lo; *(LAS u32x4*)(tb + 16) = hi;
      softmax16h(pk[1], lo, hi, [&](auto ii) NOPK {
        constexpr int i = decltype(ii)::v;
        if constexpr (i < 2) { SCHEDB(); av = mfma32(xf[28 + 2 * i], vf[8 + 2 * i], av); av = mfma32(xf[29 + 2 * i], vf[9 + 2 * i], av); SCHEDB(); }
        if constexpr (i == 4) { SCHEDB(); *(LAS u32x4*)(sm + VT + w * 2048 + l * 16) = pack8(av, 0); SCHEDB(); }
        if constexpr (i == 5) { SCHEDB(); *(LAS u32x4*)(sm + VT + w * 2048 + 1024 + l * 16) = pack8(av, 8); SCHEDB(); }
        if constexpr (i == 6) { SCHEDB(); *(LAS u32x4*)(tb + 2048) = lo; SCHEDB(); }
      });
      *(LAS u32x4*)(tb + 2064) = hi;
    }
    STAMP(7);
    waitv<PFW>();
    BAR();
    STAMP(8);
    lptr ta = sm + PT + w * 1024 + (16 * hh + 4 * blk + q4) * 32 + pp * 8;
    lptr vb = sm + VT + hh * 1024 + (16 * (w >> 1) + i16 + 32 * blk) * 16;
    const u32 one = 0x3F803F80u;
    f32x4 kv = {}, kz = {};
#pragma unroll
    for (int s4 = 0; s4 < 4; ++s4) {
      const u32x4 a = trd2(ta + s4 * 4096, ta + s4 * 4096 + 256);
      kv = mfma16(a, lds128(vb + s4 * 2048), kv);
      kz = mfma16(a, (u32x4){one, one, one, one}, kz);
    }
    const u32 pbase = WS_KVP + (j * 16 + p) * KVS;
    st64c(rs, pbase + w * 512 + l * 8, (u32x2){pk2(kv[0], kv[1]), pk2(kv[2], kv[3])});
    st64c(rs, i16 == 0 ? pbase + 2048 + w * 32 + g4 * 8 : OOB, (u32x2){pk2(kz[0], kz[1]), pk2(kz[2], kz[3])});
    MEMB(); SCHEDB();
  }
  STAMP(9);
  f32x16 aq = {};
  {
    u32x4 wq[2][4];
    auto ldq = [&](int c) NOPK {
      lptr wsl = sm + XR + ((c < 4) ? 32768 : 0) + (c & 3) * 4096 + wlane;
#pragma unroll
      for (int ks = 0; ks < 4; ++ks) wq[c & 1][ks] = trd2(wsl + ks * 1024, wsl + ks * 1024 + 256);
    };
    ldq(0);
#pragma unroll
    for (int c = 0; c < 8; ++c) {
      if (c < 7) ldq(c + 1);
      SCHEDB();
#pragma unroll
      for (int ks = 0; ks < 4; ++ks) aq = mfma32(wq[c & 1][ks], xf[c * 4 + ks], aq);
      SCHEDB();
      if (c == KVFC) { WAITV(0); st64c(rs, l == 0 ? WS_KVF + ((j * 4 + w) * 16 + p) * 128 : OOB, ftag); STAMP(10); }
      if (c == 6) BAR();
    }
  }
  STAMP(11);
  auto issueO = [&](int i) NOPK { dma4(lb + WOB[i] + w * 4096, ov0, ov1, ov2, ov3, so, (128 * w + 32 * i) * 1024); };
  u32x4 pq[2][2], sel[2];
  const u32 kfo = WS_KVF + ((j * 4 + w) * 16 + i16) * 128;
  {
    const f32x16 zero = {};
    issueO(0);
    const f32x16 t0 = mfma32(phif[0], pack8(aq, 0), zero), t1 = mfma32(phif[1], pack8(aq, 8), zero);
    SCHEDB();
    issueO(1);
    const u32 onev = (l & 1) ? 0x3F800000u : 0x00003F80u;
#pragma unroll
    for (int cg = 0; cg < 2; ++cg)
#pragma unroll
      for (int d = 0; d < 4; ++d) sel[cg][d] = ((i16 >> 1) == 4 * cg + d) ? onev : 0u;
    PIN(sel[0]); PIN(sel[1]);
    issueO(2);
    softmax16(t0, pq[0][0], pq[0][1]); PIN(pq[0][0]); PIN(pq[0][1]);
    issueO(3);
    const u32x2 f0 = ld64c(rs, kfo);
    MEMB(); SCHEDB();
    softmax16(t1, pq[1][0], pq[1][1]); PIN(pq[1][0]); PIN(pq[1][1]);
    SCHEDB();
    STAMP(12);
    if (!flag_ok(f0, ftag)) wait_flags(rs, kfo, ftag);
    MEMB();
  }
  STAMP(13);
  {
    const u32 base = WS_KVP + j * 16 * KVS;
    u32x4 v[8];
#pragma unroll
    for (int i = 0; i < 8; ++i) v[i] = ld128c(rs, base + (4 * (i >> 1) + g4) * KVS + w * 512 + (i & 1) * 256 + i16 * 16);
    const u32x2 zv = ld64c(rs, base + (l >> 2) * KVS + 2048 + w * 32 + pp * 8);
    MEMB(); SCHEDB();
    f32x4 a = {};
#pragma unroll
    for (int i = 0; i < 8; ++i) a = mfma16(sel[i & 1], v[i], a);
    *(LAS u32x2*)(sm + KVI + w * 1024 + (pp >> 1) * 512 + (16 + (l >> 2)) * 16 + (pp & 1) * 8) = zv;
    *(LAS u32x2*)(sm + KVI + w * 1024 + hh * 512 + (2 * (l & 7) + blk) * 16 + (i16 >> 3) * 8) = (u32x2){pk2(a[0], a[1]), pk2(a[2], a[3])};
  }
  WAITV(0);
  BAR();
  STAMP(14);
  u32x4 wf[32];
  {
    const f32x16 zero = {};
    lptr ap = sm + KVI + hh * 1024 + r32 * 16;
    f32x16 t[2];
#pragma unroll
    for (int h = 0; h < 2; ++h) { t[h] = mfma32(lds128(ap + h * 2048), pq[h][0], zero); t[h] = mfma32(lds128(ap + h * 2048 + 512), pq[h][1], t[h]); }
#pragma unroll
    for (int h = 0; h < 2; ++h) {
      const float dh = ((t[h][8] + t[h][9]) + (t[h][10] + t[h][11])) + ((t[h][12] + t[h][13]) + (t[h][14] + t[h][15]));
      const auto sw2 = __builtin_amdgcn_permlane32_swap(f2u(dh), f2u(dh), false, false);
      const float r = __builtin_amdgcn_rcpf((u2f(sw2[0]) + u2f(sw2[1])) + 1e-6f);
      st128c(rs, WS_OFR + ((4 * p + w) * 32 + 2 * j + h) * 1024 + l * 16,
             (u32x4){pk2(t[h][0] * r, t[h][1] * r), pk2(t[h][2] * r, t[h][3] * r), pk2(t[h][4] * r, t[h][5] * r), pk2(t[h][6] * r, t[h][7] * r)});
    }
    MEMB(); SCHEDB();
    STAMP(15);
    lptr wb = sm + kh * 8192 + (4 * hh + q4) * 64 + blk * 32 + pp * 8;
    auto ldw = [&](int i) NOPK {
      lptr q = wb + WOB[(i >> 1) & 3] + (i >> 3) * 4096 + (i & 1) * 2048;
      wf[2 * i] = trd2(q, q + 1024); wf[2 * i + 1] = trd2(q + 512, q + 1536);
    };
#pragma unroll
    for (int i = 0; i < 16; ++i) ldw(i);
    WAITV(0);
    st64c(rs, l == 0 ? WS_OF + ((4 * p + w) * 16 + j) * 128 : OOB, ftag);
  }
  BAR();
  STAMP(16);
  if (OPRE) __builtin_amdgcn_s_sleep(OPRE);
  f32x16 acc0 = {}, acc1 = {};
  {
    const u32 obase = WS_OFR + (st * 32 + 16 * kh) * 1024 + l * 16;
    const u32 fo = WS_OF + (st * 16 + 8 * kh + (l & 7)) * 128;
    u32x4 of[16];
    wait_flags(rs, fo, ftag);
    STAMP(18);
#pragma unroll
    for (int i = 0; i < 16; ++i) of[i] = ld128c(rs, obase + i * 1024);
    MEMB(); SCHEDB();
#pragma unroll
    for (int i = 0; i < 16; ++i) { acc0 = mfma32(wf[2 * i], of[i], acc0); acc1 = mfma32(wf[2 * i + 1], of[i], acc1); }
  }
  MEMB(); SCHEDB();
  STAMP(19);
  {
    lptr rp = sm + kh * 17408 + (32 * sh + r32) * 272 + hh * 16;
#pragma unroll
    for (int g = 0; g < 4; ++g) *(LAS f32x4*)(rp + g * 32) = (f32x4){acc0[4 * g], acc0[4 * g + 1], acc0[4 * g + 2], acc0[4 * g + 3]};
#pragma unroll
    for (int g = 0; g < 4; ++g) *(LAS f32x4*)(rp + 128 + g * 32) = (f32x4){acc1[4 * g], acc1[4 * g + 1], acc1[4 * g + 2], acc1[4 * g + 3]};
    BAR();
#pragma unroll
    for (int i = 0; i < 2; ++i) {
      const int row = i * 32 + (tid >> 3), oc = tid & 7;
      lptr sp = sm + row * 272 + oc * 32;
      const f32x4 a = *(LAS f32x4*)sp + *(LAS f32x4*)(sp + 17408), bb = *(LAS f32x4*)(sp + 16) + *(LAS f32x4*)(sp + 17408 + 16);
      __builtin_amdgcn_raw_buffer_store_b128((u32x4){pk2(a[0], a[1]), pk2(a[2], a[3]), pk2(bb[0], bb[1]), pk2(bb[2], bb[3])}, ro, (64 * rt + row) * 2048 + ct * 128 + oc * 16, 0, OAUX);
    }
  }
  st32c(rs, (b == 0 && tid == 0) ? WS_EPOCH : OOB, epoch + 1);
  STAMP(20);
  STAMPS_OUT();
}

DI void prologue_B(u32 lb, const int b, const char* u, const char* Are, const char* Aim, const char* Bre, const char* Bim, const char* Cre, const char* Cim) {
  const int gam = b & 7, idx = b >> 3;
  const int l = threadIdx.x & 63, w = __builtin_amdgcn_readfirstlane(threadIdx.x >> 6);
  const int ar = l >> 3;
  const char* pa = ar < 2 ? (ar ? Aim : Are) : (ar < 4 ? ((ar & 1) ? Bim : Bre) : ((ar & 1) ? Cim : Cre));
  pa += (64 * gam + 2 * idx) * 256 + w * 128 + (l & 7) * 16;
  asm volatile("s_mov_b32 m0, %0\n\ts_nop 0\n\tglobal_load_lds_dwordx4 %1, off" :: "s"(lb + RAW + w * 1024), "v"(pa) : "memory");
  const u32 uo = (l >> 3) * 1024 + (((l & 7) ^ (2 * (l >> 5))) * 16);
  setm0(lb + UT + w * 2048);
  const u32x4 su = mk_srd(u, 2048 * 1024);
  const u32 so = (64 * idx + 16 * w) * 1024 + gam * 128;
  dmao<0>(uo, su, so); dmao<1024>((uo ^ 64) + 7168, su, so);
}

#undef PROBE_SEL
#define PROBE_SEL -1
DI void role_B(lptr sm, const int b, const char* ldt, const char* Dsk, char* out, char* ws DBG_ARGS) {
  const int gam = b & 7, idx = b >> 3;
  const int tid = threadIdx.x;
  const int l = tid & 63, w = __builtin_amdgcn_readfirstlane(tid >> 6);
  const int g4 = l >> 4, i16 = l & 15, q4 = i16 >> 2, pp = l & 3, c = w >> 1, hs = w & 1;
  const RSRC rs = __builtin_amdgcn_make_buffer_rsrc(ws, 0, WS_END, 0x00020000);
  const RSRC ro = __builtin_amdgcn_make_buffer_rsrc(out, 0, 4u << 20, 0x00020000);
  STAMPS_DECL; STAMP(1);
  u32 epoch_, ld2_, dk2_, epa_;
  asm volatile("s_load_dword %0, %1, 0x0" : "=s"(epa_) : "s"(ws) : "memory");
  asm volatile("s_load_dword %0, %3, %4\n\ts_load_dword %1, %5, %7\n\ts_load_dword %2, %6, %7"
               : "=&s"(epoch_), "=&s"(ld2_), "=&s"(dk2_) : "s"(ws), "s"(WS_EPOCH + 128 * (1 + gam)), "s"(ldt), "s"(Dsk), "s"((64 * gam + 2 * idx) * 2) : "memory");
  lptr kb = sm + KL + c * KST + 40;
  lptr dummy = sm + KL + 2 * KST + l * 16;
  lptr zp = tid < 128 ? sm + UP + (tid >> 3) * UST + 16 + (tid & 7) * 16 : (tid < 136 ? sm + KL + ((tid >> 2) & 1) * KST + (tid & 3) * 16 : dummy);
  *(LAS u32x4*)zp = (u32x4){0, 0, 0, 0};
  const int beta = q4 == 0 ? pp : (q4 == 1 ? 256 + 2 * pp : (pp < 2 ? 256 * q4 + 4 * pp : 2 * q4 - 2 + pp));
  const bool first = (i16 == 0) && (hs == 0), lane0 = first && g4 == 0;
  lptr e1 = kb + (8 * i16 + 128 * hs + 256 * g4) * 2;
  lptr e2 = g4 == 0 ? dummy : e1 + 8;
  lptr e3 = g4 < 2 ? dummy : e1 - 508 * g4;
  lptr e4 = g4 < 2 ? (lane0 ? kb + 1022 * 2 : dummy) : ((first && g4 == 2) ? dummy : e1 - 4);
  lptr e5 = g4 == 1 ? e1 + 12 : ((first && g4 == 2) ? e1 - 2 : dummy);
  lptr e6 = (g4 == 1 && !first) ? e1 - 2 : dummy;
  lptr e7 = lane0 ? dummy : e1 + 2040;
  lptr e8 = lane0 ? kb + 1024 * 2 : ((l == 63 && hs == 1) ? kb + 2044 * 2 : dummy);
  PIN(e1); PIN(e2); PIN(e3); PIN(e4); PIN(e5); PIN(e6); PIN(e7); PIN(e8);
  u32 epoch, ld2, dk2;
  asm volatile("s_waitcnt vmcnt(0) lgkmcnt(0)\n\ts_barrier\n\ts_mov_b32 %0, %3\n\ts_mov_b32 %1, %4\n\ts_mov_b32 %2, %5"
               : "=&s"(epoch), "=&s"(ld2), "=&s"(dk2) : "s"(epoch_), "s"(ld2_), "s"(dk2_) : "memory");
  u32 epa; asm volatile("s_mov_b32 %0, %1" : "=s"(epa) : "s"(epa_));
  STAMP(2);
  const u32 tag = mk_tag(epoch);
  const u32x2 ftag = {tag, mk_tag2(epoch)};
  float a2, brev, ksum; f32x2 cc, m0, m1;
  u32x4 tv[2];
  {
#pragma unroll
    for (int e = 0; e < 2; ++e) {
      lptr a = sm + UT + (32 * (g4 >> 1) + 4 * q4 + 2 * e + (g4 & 1)) * 128 + (((2 * w + (pp >> 1)) ^ (2 * q4)) * 16) + (pp & 1) * 8;
      tv[e] = trd2(a, a + 16 * 128);
    }
    lptr rp = sm + RAW + w * 1024 + l * 2;
    const float are = bf1(*(LAS u16*)rp), aim = bf1(*(LAS u16*)(rp + 128)), bre = bf1(*(LAS u16*)(rp + 256)), bim = bf1(*(LAS u16*)(rp + 384));
    const float cre = bf1(*(LAS u16*)(rp + 512)), cim = bf1(*(LAS u16*)(rp + 640));
    float dt = rbf(__builtin_amdgcn_exp2f((c ? bf_hi(ld2) : bf_lo(ld2)) * 1.4426950408889634f));
    PIN(dt);
    SCHEDB();
#pragma unroll
    for (int e = 0; e < 2; ++e) *(LAS u32x4*)(sm + YS + (16 * w + i16) * 144 + (4 * e + 2 * (g4 & 1) + (g4 >> 1)) * 16) = tv[e];
#pragma unroll
    for (int e = 0; e < 2; ++e) tv[e] = lds128(sm + YS + (16 * w + 8 * e + (l >> 3)) * 144 + (l & 7) * 16);
    SCHEDB();
    cc = cmul((f32x2){cre, cim}, (f32x2){dt * bre, dt * bim});
    a2 = (dt * are) * 1.4426950408889634f; brev = (dt * aim) * 0.15915494309189535f;
    m0 = cexp_s(a2, brev, 128.f); m1 = cexp_s(a2, brev, 1024.f);
    PIN(a2); PIN(brev); PIN(cc); PIN(m0); PIN(m1);
    SCHEDB();
#pragma unroll
    for (int e = 0; e < 2; ++e) st128c(rs, WS_UX + (64 * gam + 16 * w + 8 * e + (l >> 3)) * 4096 + idx * 128 + (l & 7) * 16, tv[e]);
    const f32x2 g = cmul(cc, m1);
    ksum = __builtin_fmaf(-g.y, m1.y, g.x * m1.x);
    ksum = addd<0x111, 0xf>(ksum); ksum = addd<0x112, 0xf>(ksum); ksum = addd<0x114, 0xf>(ksum); ksum = addd<0x118, 0xf>(ksum);
    ksum = addd<0x142, 0xa>(ksum); ksum = addd<0x143, 0xc>(ksum);
    PIN(ksum);
  }
  MEMB(); SCHEDB();
  STAMP(3);
  {
    f32x4 dA = {}, dB = {}, dC = {}, dD = {};
    const float sA = (float)(8 * i16), sB = (float)beta;
    const u32 ufo = uf_off(gam + 8 * (8 * w + (l & 7)), idx >> 3);
    auto kgen = [&](auto kk) NOPK {
      constexpr int ks = decltype(kk)::v;
      u32x4 o1, o2, b1, b2;
      auto elem = [&](auto ee) NOPK {
        constexpr int e = decltype(ee)::v, I = 4 * ks + e;
        const f32x2 ea_ = cexp_b<I>(a2, brev, sA), eb = cexp_b<I>(a2, brev, sB);
        const f32x2 v1 = {fnmab<I>(cc.y, ea_.y, mulb<I>(cc.x, ea_.x)), fnmab<I>(cc.y, ea_.x, mulb<I>(cc.x, -ea_.y))};
        const f32x2 v2 = {fmab<I>(m0.y, v1.y, mulb<I>(m0.x, v1.x)), fnmab<I>(m0.y, v1.x, mulb<I>(m0.x, v1.y))};
        const f32x2 w2 = {fnmab<I>(m1.y, eb.y, mulb<I>(m1.x, eb.x)), fmab<I>(m1.y, eb.x, mulb<I>(m1.x, eb.y))};
        o1[e] = pk2(v1.x, v1.y); o2[e] = pk2(v2.x, v2.y); b1[e] = pk2(eb.x, eb.y); b2[e] = pk2(w2.x, w2.y);
      };
      elem(IC<0>{}); elem(IC<1>{}); elem(IC<2>{}); elem(IC<3>{});
      dA = mfma16(b1, o1, dA); dB = mfma16(b1, o2, dB); dC = mfma16(b2, o1, dC); dD = mfma16(b2, o2, dD);
      SCHEDB();
    };
    kgen(IC<0>{});
    if (UPUB == 1) kgen(IC<1>{});
    WAITV(0); st64c(rs, l == 0 ? uf_off(b, w) : OOB, ftag);
    STAMP(4);
    MEMB(); SCHEDB();
    if (UPUB == 0) kgen(IC<1>{});
    kgen(IC<2>{}); kgen(IC<3>{});
    STAMP(5);
    __builtin_amdgcn_s_setprio(BP1);
    lptr xc = sm + XCH + l * 16;
    *(LAS f32x4*)(xc + w * 1024) = hs ? dA : dB;
    *(LAS f32x2*)(xc + w * 1024 + 4096) = (f32x2){hs ? dC[0] : dD[0], ksum};
    BAR();
    f32x4 dt_ = (hs ? dB : dA) + *(LAS f32x4*)(xc + (w ^ 1) * 1024);
    const f32x2 dr = (f32x2){hs ? dD[0] : dC[0], ksum} + *(LAS f32x2*)(xc + (w ^ 1) * 1024 + 4096);
    const u32x2 f0 = ld64c(rs, ufo);
    STAMP(6);
    dt_[0] += lane0 ? (c ? bf_hi(dk2) : bf_lo(dk2)) : 0.f;
    {
      u32 p00 = pk2(dt_[0], dt_[0]), p01 = pk2(dt_[0], dt_[1]), p11 = pk2(dt_[1], dt_[1]), p12 = pk2(dt_[1], dt_[2]);
      u32 p22 = pk2(dt_[2], dt_[2]), p23 = pk2(dt_[2], dt_[3]);
      const float rp_ = __builtin_bit_cast(float, __builtin_amdgcn_update_dpp(0, __builtin_bit_cast(int, dr.x), 0xA0, 0xf, 0xf, true));
      u32 rr = pk2(dr.x, dr.x), rf = pk2(rp_, dr.x), kk = pk2(dr.y, dr.y);
      PIN(p00); PIN(p01); PIN(p11); PIN(p12); PIN(p22); PIN(p23); PIN(rr); PIN(kk);
      const u32 t2 = g4 == 1 ? p12 : p01, t8 = l == 0 ? rr : kk;
      *(LAS u32x4*)e7 = (u32x4){rf, rr, rr, rr};
      *(LAS u32x2*)e1 = (u32x2){g4 == 0 ? p01 : p00, g4 == 0 ? p23 : t2};
      *(LAS u32*)e2 = g4 == 1 ? p22 : p11;
      *(LAS u32*)e3 = p23;
      *(LAS u32*)e4 = g4 == 0 ? rr : p00;
      *(LAS u16*)e5 = (u16)(g4 == 1 ? p23 >> 16 : p00);
      *(LAS u16*)e6 = (u16)p00;
      *(LAS u32x2*)e8 = (u32x2){t8, t8};
    }
    MEMB(); SCHEDB();
    STAMP(7);
    if (!flag_ok(f0, ftag)) wait_flags(rs, ufo, ftag);
    MEMB();
    STAMP(8);
    u32x4 uv[2];
#pragma unroll
    for (int e = 0; e < 2; ++e) uv[e] = ld128c(rs, WS_UX + (64 * gam + 2 * idx + e) * 4096 + tid * 16);
#pragma unroll
    for (int e = 0; e < 2; ++e) {
      lptr q2 = sm + UP + (e * 4 + ((tid >> 1) & 3)) * UST + 128 + (tid >> 3) * 32 + (tid & 1) * 16;
      *(LAS u32x4*)q2 = uv[e];
      *(volatile LAS u32x2*)(q2 + UC2 + 8) = (u32x2){uv[e].x, uv[e].y};
      *(volatile LAS u32x2*)(q2 + UC2 + 16) = (u32x2){uv[e].z, uv[e].w};
    }
  }
  BAR();
  *(LAS f32x4*)(tid < 2 ? sm + CB + tid * CST : dummy) = (f32x4){};
  st32c(rs, (idx == 0 && tid == 0) ? WS_EPOCH + 128 * (1 + gam) : OOB, epoch + 1);
  STAMP(9);
  __builtin_amdgcn_s_setprio(BP1);
  if (PACE3) { pace_wait(rs, WS_PF + b * 128, mk_tag(epa)); if (PACE3 > 1) __builtin_amdgcn_s_sleep(PACE3); }
  if (PACE2) {
    const int p = (gam >> 1) * 4 + (idx >> 3), j = (gam & 1) * 8 + (idx & 7);
    pace_wait(rs, WS_KVF + ((j * 4 + w) * 16 + p) * 128, mk_tag(epa));
    if (PACE2 > 1) __builtin_amdgcn_s_sleep(PACE2);
  }
  {
    f32x4 acc[4][4];
    lptr ka = kb + (4 * pp - 4 * (8 * g4 + q4) + 112) * 2;
    lptr ub = sm + UP + c * 4 * UST + 128 + (4 * i16 + 8 * g4 - 60) * 2 + ((l & 1) ? 0 : UC2 + 8);
    if (hs == 0) conv_tiles<0, 3, 4, 7>(ka, ub, UST, acc); else conv_tiles<1, 2, 5, 6>(ka, ub, UST, acc);
    STAMP(10);
    __builtin_amdgcn_s_setprio(BP2);
    lptr cb = sm + CB + c * CST + 16;
    f32x4 y[4];
#pragma unroll
    for (int t = 0; t < 4; ++t) {
      const int tau = 2 * t + ((t & 1) ^ hs);
      const f32x4 *z = acc[t];
      *(LAS f32x4*)(cb + (64 * tau + l) * 16) = (f32x4){(z[1][3] + z[2][2]) + z[3][1], z[2][3] + z[3][2], z[3][3], z[3][3]};
    }
#pragma unroll
    for (int t = 0; t < 4; ++t) {
      const f32x4 *z = acc[t];
      y[t] = (f32x4){z[0][0], z[0][1] + z[1][0], (z[0][2] + z[1][1]) + z[2][0], (z[0][3] + z[1][2]) + (z[2][1] + z[3][0])};
      PIN(y[t]);
    }
    BAR();
    STAMP(11);
    const int src = g4 > 0 ? l - 16 : (i16 > 0 ? 47 + i16 : -1);
    u32x2 yp[4];
#pragma unroll
    for (int t = 0; t < 4; ++t) {
      const int tau = 2 * t + ((t & 1) ^ hs);
      const f32x4 r = *(LAS f32x4*)(cb + (64 * tau + src) * 16);
      yp[t] = (u32x2){pk2(y[t][0] + r[0], y[t][1] + r[1]), pk2(y[t][2] + r[2], y[t][3])};
    }
#pragma unroll
    for (int t = 0; t < 4; t += 2) {
      const auto s0 = __builtin_amdgcn_permlane16_swap(yp[t].x, yp[t + 1].x, false, false);
      const auto s1 = __builtin_amdgcn_permlane16_swap(yp[t].y, yp[t + 1].y, false, false);
      const int tau = (g4 & 1) ? 2 * t + 3 - hs : 2 * t + hs;
      st128c(rs, WS_YT + (64 * gam + 2 * idx + c) * 4096 + tau * 512 + q4 * 128 + (g4 >> 1) * 64 + pp * 16, (u32x4){s0[0], s1[0], s0[1], s1[1]});
    }
  }
  MEMB(); SCHEDB();
  STAMP(12);
  WAITV(0);
  st64c(rs, l == 0 ? WS_YF + ((gam * 4 + w) * 32 + idx) * 128 : OOB, ftag);
  STAMP(13);
  __builtin_amdgcn_s_sleep(YPRE);
  {
    const int tl = idx >> 2, hsn = ((tl >> 1) ^ tl) & 1;
    wait_flags(rs, WS_YF + ((gam * 4 + 2 * ((l >> 3) & 1) + hsn) * 32 + 8 * w + (l & 7)) * 128, ftag);
  }
  STAMP(14);
  {
    u32x4 v[2];
#pragma unroll
    for (int e = 0; e < 2; ++e) v[e] = ld128c(rs, WS_YT + (64 * gam + 16 * w + 8 * e + (l >> 3)) * 4096 + idx * 128 + (l & 7) * 16);
#pragma unroll
    for (int e = 0; e < 2; ++e) *(LAS u32x4*)(sm + YS + (16 * w + 8 * e + (l >> 3)) * 144 + (2 * pp + ((l >> 2) & 1)) * 16) = v[e];
  }
  BAR();
#pragma unroll
  for (int e = 0; e < 2; ++e) {
    lptr a = sm + YS + (8 * (4 * e + g4) + q4) * 144 + w * 32 + pp * 8;
    *(LAS u32x4*)(sm + UT + (16 * w + i16) * 144 + (4 * e + g4) * 16) = trd2(a, a + 4 * 144);
  }
#pragma unroll
  for (int e = 0; e < 2; ++e)
    __builtin_amdgcn_raw_buffer_store_b128(lds128(sm + UT + (16 * w + 8 * e + (l >> 3)) * 144 + (l & 7) * 16), ro, (64 * idx + 16 * w + 8 * e + (l >> 3)) * 2048 + 1024 + gam * 128 + (l & 7) * 16, 0, OAUX);
  STAMP(15);
  STAMPS_OUT();
}

#define RUN_A 1
#define RUN_B 1
__global__ __launch_bounds__(256, 2) NOPK void fused_kernel(const char* x, const char* W, const char* wphi, const char* wo, const char* u,
                                                            const char* Are, const char* Aim, const char* Bre, const char* Bim, const char* Cre, const char* Cim,
                                                            const char* ldt, const char* Dsk, char* out, char* ws) {
  extern __shared__ __attribute__((aligned(16))) char smem[];
  lptr sm = (lptr)smem;
  const u64 tbeg_ = __builtin_amdgcn_s_memrealtime();
  asm volatile("" :: "s"(x), "s"(W), "s"(wphi), "s"(wo), "s"(u), "s"(Are), "s"(Aim), "s"(Bre), "s"(Bim), "s"(Cre), "s"(Cim), "s"(ldt), "s"(Dsk), "s"(out), "s"(ws));
  const int b = blockIdx.x & 255;
  u32 isA = blockIdx.x >> 8;
  __builtin_amdgcn_s_setprio(3);
  if (!isA) prologue_B((u32)(size_t)sm, b, u, Are, Aim, Bre, Bim, Cre, Cim);
  asm volatile("" : "+s"(isA));
  if (__builtin_expect(isA, 1)) { __builtin_amdgcn_s_setprio(APRIO); role_A(sm, b, x, W, wphi, wo, out, ws DBG_PASS); }
  else { __builtin_amdgcn_s_setprio(BP0); role_B(sm, b, ldt, Dsk, out, ws DBG_PASS); }
}

extern "C" void kernel_launch(void* const* d_in, const int* in_sizes, int n_in, void* d_out, int out_size, void* d_ws, size_t ws_size,
                              hipStream_t stream) {
  (void)hipFuncSetAttribute((const void*)fused_kernel, hipFuncAttributeMaxDynamicSharedMemorySize, LDS_BYTES);
  fused_kernel<<<512, 256, LDS_BYTES, stream>>>((const char*)d_in[0], (const char*)d_in[1], (const char*)d_in[2], (const char*)d_in[3], (const char*)d_in[4],
                                                (const char*)d_in[5], (const char*)d_in[6], (const char*)d_in[7], (const char*)d_in[8], (const char*)d_in[9], (const char*)d_in[10],
                                                (const char*)d_in[11], (const char*)d_in[12], (char*)d_out, (char*)d_ws);
}
```

```cpp
#include <hip/hip_runtime.h>
#include <hip/hip_bf16.h>

typedef unsigned int u32;
typedef unsigned long long u64;
typedef unsigned short u16;
typedef short s16x4 __attribute__((ext_vector_type(4)));
typedef short s16x8 __attribute__((ext_vector_type(8)));
typedef __bf16 bf16x2 __attribute__((ext_vector_type(2)));
typedef __bf16 bf16x8 __attribute__((ext_vector_type(8)));
typedef float f32x2 __attribute__((ext_vector_type(2)));
typedef float f32x4 __attribute__((ext_vector_type(4)));
typedef float f32x16 __attribute__((ext_vector_type(16)));
typedef u32 u32x2 __attribute__((ext_vector_type(2)));
typedef u32 u32x4 __attribute__((ext_vector_type(4)));
#define LAS __attribute__((address_space(3)))
#define GAS __attribute__((address_space(1)))
#if defined(__HIP_DEVICE_COMPILE__)
#define NOPK __attribute__((target("no-packed-fp32-ops")))
#else
#define NOPK
#endif
#define DI __device__ __forceinline__ NOPK
typedef LAS char* lptr;

#define PSLEEP 16
#define OPRE 20
#define YPRE 20
#define UFK 1
#define APRIO 2
#define BP0 0
#define BP1 0
#define BP2 0
#define ADELAY 2
#define CONVT 0
#define KVFC 4
#define KSPLIT 3
#define OAUX 17
#define SWAP_ROLES 1
constexpr u32 WS_EPOCH = 0;
constexpr u32 WS_OF = 4096;
constexpr u32 WS_UF = WS_OF + 1024 * 128;
constexpr u32 WS_YF = WS_UF + 1024 * 128;
constexpr u32 WS_KVF = WS_YF + 1024 * 128;
constexpr u32 WS_KVP = 1u << 20;
constexpr u32 KVS = 2176;
constexpr u32 WS_OFR = 2u << 20;
constexpr u32 WS_UX = 4u << 20;
constexpr u32 WS_YT = 6u << 20;
constexpr u32 WS_END = 8u << 20;
constexpr u32 OOB = 0x7FFFFFF0u;
constexpr int LDS_BYTES = 81920;
constexpr u32 SPIN_CAP = 1u << 16;

#define STAMPS_DECL
#define STAMP(k)
#define STAMPS_OUT()

#define BAR() asm volatile("s_waitcnt lgkmcnt(0)\n\ts_barrier" ::: "memory")
#define WAITV(n) asm volatile("s_waitcnt vmcnt(" #n ")" ::: "memory")
#define MEMB() asm volatile("" ::: "memory")
#define SCHEDB() __builtin_amdgcn_sched_barrier(0)

DI float u2f(u32 v) { return __builtin_bit_cast(float, v); }
DI u32 f2u(float v) { return __builtin_bit_cast(u32, v); }
DI bool all_lanes(bool c) { return __builtin_amdgcn_ballot_w64(!c) == 0; }
DI u32 pk2(float a, float b) { f32x2 v = {a, b}; return __builtin_bit_cast(u32, __builtin_convertvector(v, bf16x2)); }
DI float bf_lo(u32 v) { return u2f(v << 16); }
DI float bf_hi(u32 v) { return u2f(v & 0xffff0000u); }
DI float bf1(u16 v) { return u2f((u32)v << 16); }
DI float rbf(float a) { return bf_lo(pk2(a, a)); }
DI u32x2 trd(lptr p) { return __builtin_bit_cast(u32x2, __builtin_amdgcn_ds_read_tr16_b64_v4i16((LAS s16x4*)p)); }
DI u32x4 trd2(lptr p0, lptr p1) { u32x2 a = trd(p0), b = trd(p1); return (u32x4){a.x, a.y, b.x, b.y}; }
DI u32x4 lds128(lptr p) { return *(LAS u32x4*)p; }
DI u32x2 lds64(lptr p) { return *(volatile LAS u32x2*)p; }
DI f32x16 mfma32(u32x4 a, u32x4 b, f32x16 c) { return __builtin_amdgcn_mfma_f32_32x32x16_bf16(__builtin_bit_cast(bf16x8, a), __builtin_bit_cast(bf16x8, b), c, 0, 0, 0); }
DI f32x4 mfma16(u32x4 a, u32x4 b, f32x4 c) { return __builtin_amdgcn_mfma_f32_16x16x32_bf16(__builtin_bit_cast(bf16x8, a), __builtin_bit_cast(bf16x8, b), c, 0, 0, 0); }
DI u32x4 mk_srd(const void* p, u32 bytes) { const u64 a = (u64)p; return (u32x4){(u32)a, (u32)(a >> 32) & 0xffffu, bytes, 0x00020000u}; }
DI void dma1(u32 lds, u32 v0, u32x4 srd, u32 soff) {
  asm volatile("s_mov_b32 m0, %0\n\ts_nop 0\n\tbuffer_load_dwordx4 %1, %2, %3 offen lds" :: "s"(lds), "v"(v0), "s"(srd), "s"(soff) : "memory");
}
DI void dma2(u32 lds, u32 v0, u32x4 srd, u32 soff) {
  asm volatile("s_mov_b32 m0, %0\n\ts_nop 0\n\tbuffer_load_dwordx4 %1, %2, %3 offen lds\n\tbuffer_load_dwordx4 %1, %2, %3 offen offset:1024 lds"
               :: "s"(lds), "v"(v0), "s"(srd), "s"(soff) : "memory");
}
DI void dma4(u32 lds, u32 v0, u32 v1, u32 v2, u32 v3, u32x4 srd, u32 soff) {
  asm volatile("s_mov_b32 m0, %0\n\ts_nop 0\n\tbuffer_load_dwordx4 %1, %5, %6 offen lds\n\tbuffer_load_dwordx4 %2, %5, %6 offen offset:1024 lds\n\t"
               "buffer_load_dwordx4 %3, %5, %6 offen offset:2048 lds\n\tbuffer_load_dwordx4 %4, %5, %6 offen offset:3072 lds"
               :: "s"(lds), "v"(v0), "v"(v1), "v"(v2), "v"(v3), "s"(srd), "s"(soff) : "memory");
}
DI void setm0(u32 lds) { asm volatile("s_mov_b32 m0, %0\n\ts_nop 0" :: "s"(lds) : "memory"); }
template <int OFF> DI void dmao(u32 v, u32x4 srd, u32 soff) {
  asm volatile("buffer_load_dwordx4 %0, %1, %2 offen offset:%3 lds" :: "v"(v), "s"(srd), "s"(soff), "i"(OFF) : "memory");
}
#define PIN(x) asm volatile("" : "+v"(x))
DI u32 mk_tag(u32 e) { const u32 t = (e + 0x1B4E0193u) * 0x2F4A7C15u; return (t & 0x3FFFu) | ((t & 0x1FFFC000u) << 1) | ((t & 0x20000000u) << 2); }
DI u32 mk_tag2(u32 e) { return (e ^ 0x5BD1E995u) * 0x9E3779B1u; }
#define RSRC __amdgpu_buffer_rsrc_t
DI void st128c(RSRC r, u32 off, u32x4 v) { __builtin_amdgcn_raw_buffer_store_b128(v, r, off, 0, 16); }
DI void st64c(RSRC r, u32 off, u32x2 v) { __builtin_amdgcn_raw_buffer_store_b64(v, r, off, 0, 16); }
DI void st32c(RSRC r, u32 off, u32 v) { __builtin_amdgcn_raw_buffer_store_b32(v, r, off, 0, 16); }
DI u32x4 ld128c(RSRC r, u32 off) { return __builtin_amdgcn_raw_buffer_load_b128(r, off, 0, 16); }
DI u32x2 ld64c(RSRC r, u32 off) { return __builtin_amdgcn_raw_buffer_load_b64(r, off, 0, 16); }
DI bool flag_ok(u32x2 f, u32x2 tg) { return all_lanes(f.x == tg.x && f.y == tg.y); }
DI void wait_flags(RSRC r, u32 off, u32x2 tg) {
#pragma unroll 1
  for (u32 spins = 0; spins < SPIN_CAP; ++spins) {
    MEMB();
    if (flag_ok(ld64c(r, off), tg)) break;
    __builtin_amdgcn_s_sleep(PSLEEP);
  }
  MEMB();
}
DI u32 tag_diff(u32 d, u32x4 v, u32 tag) { return d | (v.y ^ tag) | (v.w ^ tag); }
DI u32 uf_off(int blk, int w) { return WS_UF + (((blk & 7) * 4 + w) * 32 + (blk >> 3)) * 128; }

struct GB { u32 wr, rd; };
template <int TARGET> DI void gbar(const GB& g) {
  u32 t;
  asm volatile("v_mov_b32 %0, %3\n\ts_waitcnt lgkmcnt(0)\n\tds_write_b32 %1, %0\n"
               "1:\n\tds_read_b32 %0, %2\n\ts_waitcnt lgkmcnt(0)\n\tv_cmp_gt_u32 vcc, %3, %0\n\ts_cbranch_vccnz 1b"
               : "=&v"(t) : "v"(g.wr), "v"(g.rd), "i"(TARGET) : "memory", "vcc");
}

template <int TARGET> DI void gpost(const GB& g) {
  u32 t;
  asm volatile("v_mov_b32 %0, %2\n\tds_write_b32 %1, %0" : "=&v"(t) : "v"(g.wr), "i"(TARGET) : "memory");
}
template <int TARGET> DI void gcheck(const GB& g) {
  u32 t;
  asm volatile("1:\n\tds_read_b32 %0, %1\n\ts_waitcnt lgkmcnt(0)\n\tv_cmp_gt_u32 vcc, %2, %0\n\ts_cbranch_vccnz 1b"
               : "=&v"(t) : "v"(g.rd), "i"(TARGET) : "memory", "vcc");
}
template <int TARGET> DI void gverify(const GB& g, u32 pv) {
  asm volatile("v_cmp_gt_u32 vcc, %2, %0\n\ts_cbranch_vccz 2f\n"
               "1:\n\tds_read_b32 %0, %1\n\ts_waitcnt lgkmcnt(0)\n\tv_cmp_gt_u32 vcc, %2, %0\n\ts_cbranch_vccnz 1b\n2:"
               : "+v"(pv) : "v"(g.rd), "i"(TARGET) : "memory", "vcc");
}
template <int N> struct IC { static constexpr int v = N; };
template <int N> DI void waitv() { asm volatile("s_waitcnt vmcnt(%0)" :: "i"(N) : "memory"); }

DI float max3f(float a, float b, float c) { float r; asm("v_max3_f32 %0, %1, %2, %3" : "=v"(r) : "v"(a), "v"(b), "v"(c)); return r; }
template <class H> DI void softmax16h(const f32x16& p, u32x4& lo, u32x4& hi, H hook) {
  const float m0 = __builtin_fmaxf(p[0], p[1]);
  const float m1 = __builtin_fmaxf(p[14], p[15]);
  float ma = max3f(m0, p[2], p[3]), mb_ = max3f(m1, p[4], p[5]);
  ma = max3f(ma, p[6], p[7]); mb_ = max3f(mb_, p[8], p[9]);
  ma = max3f(ma, p[10], p[11]); mb_ = max3f(mb_, p[12], p[13]);
  const float L2E = 1.4426950408889634f;
  const float mb = max3f(ma, mb_, mb_) * -L2E;
  hook(IC<0>{});
  float e[16];
#pragma unroll
  for (int i = 0; i < 4; ++i) e[i] = __builtin_amdgcn_exp2f(__builtin_fmaf(p[i], L2E, mb));
  hook(IC<1>{});
#pragma unroll
  for (int i = 4; i < 8; ++i) e[i] = __builtin_amdgcn_exp2f(__builtin_fmaf(p[i], L2E, mb));
  hook(IC<2>{});
#pragma unroll
  for (int i = 8; i < 12; ++i) e[i] = __builtin_amdgcn_exp2f(__builtin_fmaf(p[i], L2E, mb));
  hook(IC<3>{});
#pragma unroll
  for (int i = 12; i < 16; ++i) e[i] = __builtin_amdgcn_exp2f(__builtin_fmaf(p[i], L2E, mb));
  hook(IC<4>{});
  const float s0 = ((e[0] + e[1]) + (e[2] + e[3])) + ((e[4] + e[5]) + (e[6] + e[7]));
  const float s1 = ((e[8] + e[9]) + (e[10] + e[11])) + ((e[12] + e[13]) + (e[14] + e[15]));
  const float r = __builtin_amdgcn_rcpf(s0 + s1);
  hook(IC<5>{});
  lo = (u32x4){pk2(e[0] * r, e[1] * r), pk2(e[2] * r, e[3] * r), pk2(e[4] * r, e[5] * r), pk2(e[6] * r, e[7] * r)};
  hook(IC<6>{});
  hi = (u32x4){pk2(e[8] * r, e[9] * r), pk2(e[10] * r, e[11] * r), pk2(e[12] * r, e[13] * r), pk2(e[14] * r, e[15] * r)};
  hook(IC<7>{});
}
DI void softmax16(const f32x16& p, u32x4& lo, u32x4& hi) { softmax16h(p, lo, hi, [](auto) NOPK {}); }
DI u32x4 pack8(const f32x16& a, int o) { return (u32x4){pk2(a[o], a[o + 1]), pk2(a[o + 2], a[o + 3]), pk2(a[o + 4], a[o + 5]), pk2(a[o + 6], a[o + 7])}; }

DI f32x2 cmul(f32x2 a, f32x2 b) { return (f32x2){__builtin_fmaf(-a.y, b.y, a.x * b.x), __builtin_fmaf(a.y, b.x, a.x * b.y)}; }
DI f32x2 cmulc(f32x2 a, f32x2 b) { return (f32x2){__builtin_fmaf(a.y, b.y, a.x * b.x), __builtin_fmaf(-a.x, b.y, a.y * b.x)}; }
DI f32x2 cexp_s(float a2, float brev, float s) {
  const float m = __builtin_amdgcn_exp2f(a2 * s);
  const float th = __builtin_amdgcn_fractf(brev * s);
  return (f32x2){m * __builtin_amdgcn_cosf(th), m * __builtin_amdgcn_sinf(th)};
}
template <int I> DI float mulb(float q, float x) {
  return __builtin_bit_cast(float, __builtin_amdgcn_update_dpp(0, __builtin_bit_cast(int, q), 0x150 + I, 0xf, 0xf, true)) * x;
}
template <int I> DI float fmab(float q, float x, float acc) {
  asm("v_fmac_f32_dpp %0, %1, %2 row_newbcast:%3 row_mask:0xf bank_mask:0xf" : "+v"(acc) : "v"(q), "v"(x), "i"(I));
  return acc;
}
template <int I> DI float fnmab(float q, float x, float acc) {
  asm("v_fmac_f32_dpp %0, -%1, %2 row_newbcast:%3 row_mask:0xf bank_mask:0xf" : "+v"(acc) : "v"(q), "v"(x), "i"(I));
  return acc;
}
template <int I> DI f32x2 cexp_b(float a2, float brev, float s) {
  const float m = __builtin_amdgcn_exp2f(mulb<I>(a2, s));
  const float th = __builtin_amdgcn_fractf(mulb<I>(brev, s));
  return (f32x2){m * __builtin_amdgcn_cosf(th), m * __builtin_amdgcn_sinf(th)};
}
template <int CTRL, int RM> DI float addd(float v) {
  return v + __builtin_bit_cast(float, __builtin_amdgcn_update_dpp(0, __builtin_bit_cast(int, v), CTRL, RM, 0xf, true));
}

template <int T0, int T1, int T2, int T3>
DI void conv_tiles(lptr ka, lptr ub, int ust, f32x4 (&acc)[4][4]) {
  constexpr int T[4] = {T0, T1, T2, T3};
  constexpr int NB = 2 * T3 + 2, PF = 8;
  u32x4 af[NB], bfr[4 * NB];
  bool have[NB] = {};
  auto lda = [&](int m) NOPK { af[m] = trd2(ka + m * 256, ka + m * 256 - 32); };
  auto ldb = [&](int n) NOPK {
    lptr q = ub + (n / NB) * ust + (n % NB) * 64;
    bfr[n] = lds128(q);
  };
  auto need = [&](int n) NOPK {
    ldb(n);
    if (n < NB) {
#pragma unroll
      for (int t = 0; t < 4; ++t) { const int m = 2 * T[t] + 1 - n; if (m >= 0 && !have[m]) { lda(m); have[m] = true; } }
    }
  };
  need(0); need(1);
#pragma unroll
  for (int n = 0; n < 4 * NB; ++n) {
    if (n < PF - 2) { need(2 * n + 2); need(2 * n + 3); }
    else if (n + PF < 4 * NB) need(n + PF);
    SCHEDB();
    const int rho = n / NB, be = n % NB;
#pragma unroll
    for (int t = 0; t < 4; ++t) {
      const int m = 2 * T[t] + 1 - be;
      if (m >= 0) acc[t][rho] = mfma16(af[m], bfr[n], be == 0 ? (f32x4){} : acc[t][rho]);
    }
    SCHEDB();
  }
}

#define EARLYV 1
#define PFW 1
#define PROG 0
#define UPUB 1
#define ROT ((idx & 7) + 2 * (idx >> 3))
#define DBG_ARGS
#define DBG_PASS
constexpr int XR = 0, KR = 49152, VR = 61440, PHI = 77824, KVI = KR + 8192;
constexpr int CB = 0, CST = 8208;
constexpr int XCH = 0;
constexpr int YS = 16640;
constexpr int RAW = 26112;
constexpr int UT = 36352;
constexpr int UP = 45568, UST = 1168, UC2 = 9344;
constexpr int KL = 64256, KST = 4448;

struct AAddr { u32 xv0, xv1, xv2, xv3, wv0; };
DI AAddr a_addr(int p, int j, int w, int l) {
  AAddr a;
  a.xv0 = (128 * p + 32 * w + (l >> 3)) * 1024 + ((l & 7) ^ (l >> 4)) * 16;
  a.xv1 = (a.xv0 ^ 64) + 7168; a.xv2 = a.xv0 + 2 * 7168; a.xv3 = a.xv1 + 2 * 7168;
  a.wv0 = (l >> 2) * 3072 + j * 64 + (l & 3) * 16;
  return a;
}
DI void role_A(lptr sm, const int b, const char* x, const char* W, const char* wphi, const char* wo, char* out, char* ws DBG_ARGS) {
  const int xcd = b & 7, idx = b >> 3;
  const int p = (xcd >> 1) * 4 + (idx >> 3), j = (xcd & 1) * 8 + (idx & 7);
  const int tid = threadIdx.x;
  const int l = tid & 63, w = __builtin_amdgcn_readfirstlane(tid >> 6);
  const int hh = l >> 5, blk = (l >> 4) & 1, i16 = l & 15, g4 = l >> 4, q4 = i16 >> 2, pp = l & 3, r32 = l & 31;
  const u32 lb = (u32)(size_t)sm;
  u32 epoch_;
  asm volatile("s_load_dword %0, %1, 0x0" : "=s"(epoch_) : "s"(ws) : "memory");
  const int r0 = ROT;
  const u32x4 sx = mk_srd(x, 2048 * 1024), sw = mk_srd(W, 512 * 3072);
  const AAddr ad = a_addr(p, j, w, l);
  auto vslot = [&](int c) NOPK { return c < 7 ? VR + (c & 3) * 4096 : KR + 8192; };
  auto issueW = [&](int dst, int mat, int c) NOPK { dma1(lb + dst + w * 1024, ad.wv0, sw, (((c + r0) & 7) * 64 + 16 * w) * 3072 + mat * 1024); };
#pragma unroll
  for (int c = 0; c < 3; ++c) {
    dma4(lb + XR + c * 16384 + w * 4096, ad.xv0, ad.xv1, ad.xv2, ad.xv3, sx, ((c + r0) & 7) * 128);
    if (c < 2) dma1(lb + KR + c * 4096 + w * 1024, ad.wv0, sw, (((c + r0) & 7) * 64 + 16 * w) * 3072 + 1024);
  }
  dma1(lb + PHI, l * 16, mk_srd(wphi, 32 * 512), j * 1024);
  issueW(vslot(0), 2, 0);
  {
    asm volatile("s_mov_b32 m0, %0\n\ts_nop 0\n\tbuffer_load_dword %1, %2, %3 offen lds" :: "s"(lb + PHI + 1024), "v"(ad.wv0 & ~63u), "s"(sw), "s"(((r0 & 7) * 64 + 16 * w) * 3072) : "memory");
  }
  const RSRC rs = __builtin_amdgcn_make_buffer_rsrc(ws, 0, WS_END, 0x00020000);
  const RSRC ro = __builtin_amdgcn_make_buffer_rsrc(out, 0, 4u << 20, 0x00020000);
  STAMPS_DECL; STAMP(1);
  const u32 wv1 = ad.wv0 + 48128, wv2 = ad.wv0 + 2 * 48128, wv3 = ad.wv0 + 3 * 48128;
  const int rt = xcd * 4 + (idx >> 3), ct = idx & 7;
  const int sh = w & 1, kh = w >> 1, st = 2 * rt + sh;

  const int wlane = (8 * hh + q4) * 64 + blk * 32 + pp * 8;
  const int xsw = (r32 >> 1) & 7;
  u32 epoch;
  lptr vbase = sm + KR + wlane; PIN(vbase);
  u32x4 xf[32], phif[2];
  f32x16 ak = {}, av = {};
#pragma unroll
  for (int c = 0; c < 8; ++c) {
    if (EARLYV) { if (c < 2) WAITV(12); else if (c == 2) WAITV(10); else if (c < 6) WAITV(11); else WAITV(7); }
    else { if (c == 0) WAITV(10); else if (c < 6) WAITV(11); else WAITV(7); }
    BAR();
    if (c == 0) { asm volatile("s_mov_b32 %0, %1" : "=s"(epoch) : "s"(epoch_)); STAMP(2); } if (c == 4) STAMP(3); if (c == 7) STAMP(4);
    lptr xs = sm + XR + (c % 3) * 16384 + w * 4096 + r32 * 128;
    lptr wsl = vbase + (c % 3) * 4096;
    lptr vsl = vbase + (vslot(c - 3) - KR);
    u32x4 wk[4], wv[4];
#pragma unroll
    for (int ks = 0; ks < 4; ++ks) { xf[c * 4 + ks] = lds128(xs + (((2 * ks + hh) ^ xsw) * 16)); wk[ks] = trd2(wsl + ks * 1024, wsl + ks * 1024 + 256); }
    if (c + 2 < 8) issueW(KR + ((c + 2) % 3) * 4096, 1, c + 2);
    if (c == 6) { issueW(vslot(6), 2, 6); issueW(vslot(7), 2, 7); }
    SCHEDB();
#pragma unroll
    for (int ks = 0; ks < 4; ++ks) {
      ak = mfma32(wk[ks], xf[c * 4 + ks], ak);
      if (c >= 3) wv[ks] = trd2(vsl + ks * 1024, vsl + ks * 1024 + 256);
      SCHEDB();
    }
    {
      const bool isx = c + 3 < 8, isq = c >= 6;
      const u32 so_ = isx ? (u32)(((c + 3 + r0) & 7) * 128) : (u32)(((4 * (c - 6) + w + r0) & 7) * 64 * 3072);
      if (isx || isq) setm0(lb + XR + ((isx ? c : c - 1) % 3) * 16384 + w * 4096);
      if (c >= 3) av = mfma32(xf[(c - 3) * 4], wv[0], av);
      if (isx) dmao<0>(ad.xv0, sx, so_); else if (isq) dmao<0>(ad.wv0, sw, so_);
      SCHEDB();
      if (c >= 3) av = mfma32(xf[(c - 3) * 4 + 1], wv[1], av);
      if (isx) dmao<1024>(ad.xv1, sx, so_); else if (isq) dmao<1024>(wv1, sw, so_);
      SCHEDB();
      if (c >= 3) av = mfma32(xf[(c - 3) * 4 + 2], wv[2], av);
      if (isx) dmao<2048>(ad.xv2, sx, so_); else if (isq) dmao<2048>(wv2, sw, so_);
      SCHEDB();
      if (c >= 3) av = mfma32(xf[(c - 3) * 4 + 3], wv[3], av);
      if (isx) dmao<3072>(ad.xv3, sx, so_); else if (isq) dmao<3072>(wv3, sw, so_);
      if (c < 6 && (c > 0 || !EARLYV)) issueW(vslot(c), 2, c);
    }
    if (c == 2) {
      const u32 sg = ((l >> 2) & 1) ? 0x80008000u : 0u;
      lptr pb = sm + PHI + (4 * hh + q4) * 32 + (8 * blk + 4 * (pp >> 1)) * 2;
#pragma unroll
      for (int h = 0; h < 2; ++h) { u32x4 v = trd2(pb + h * 512, pb + h * 512 + 256); phif[h] = (u32x4){v.x ^ sg, v.y ^ sg, v.z ^ sg, v.w ^ sg}; }
    }
    SCHEDB();
  }
  STAMP(5);
  const u32 tag = mk_tag(epoch);
  const u32x2 ftag = {tag, mk_tag2(epoch)};
  const u32x4 so = mk_srd(wo, 512 * 1024);
  const u32 ov0 = ((l >> 2) & 7) * 1024 + ct * 128 + hh * 64 + pp * 16;
  const u32 ov1 = ov0 + 7168, ov2 = ov0 + 2 * 7168, ov3 = ov0 + 3 * 7168;
  constexpr int WOB[4] = {VR, 16384, 32768, 0};
  {
    constexpr int PT = XR + 16384, VT = KR;
    const f32x16 zero = {};
    f32x16 pk[2];
    pk[0] = mfma32(phif[0], pack8(ak, 0), zero); pk[1] = mfma32(phif[1], pack8(ak, 8), zero);
    lptr tb = sm + PT + w * 4096 + l * 32;
    {
      const char* t = l < 32 ? wo + (idx * 128 + w * 32 + l) * 128 : out + (size_t)(l < 48 ? 64 * rt : 64 * idx) * 2048;
      asm volatile("s_mov_b32 m0, %0\n\ts_nop 0\n\tglobal_load_lds_dword %1, off" :: "s"(lb + PHI + 1024), "v"(t) : "memory");
    }
    WAITV(9);
    BAR();
    STAMP(6);
    {
      u32x4 vf[12];
      auto ldv = [&](int i) NOPK { lptr q = vbase + (vslot(5 + (i >> 2)) - KR) + (i & 3) * 1024; vf[i] = trd2(q, q + 256); };
#pragma unroll
      for (int i = 0; i < 6; ++i) ldv(i);
      SCHEDB();
      u32x4 lo, hi;
      softmax16h(pk[0], lo, hi, [&](auto ii) NOPK {
        constexpr int i = decltype(ii)::v;
        SCHEDB();
        av = mfma32(xf[20 + i], vf[i], av);
        if constexpr (i + 6 < 12) ldv(i + 6);
        SCHEDB();
      });
      *(LAS u32x4*)tb = lo; *(LAS u32x4*)(tb + 16) = hi;
      softmax16h(pk[1], lo, hi, [&](auto ii) NOPK {
        constexpr int i = decltype(ii)::v;
        if constexpr (i < 2) { SCHEDB(); av = mfma32(xf[28 + 2 * i], vf[8 + 2 * i], av); av = mfma32(xf[29 + 2 * i], vf[9 + 2 * i], av); SCHEDB(); }
        if constexpr (i == 4) { SCHEDB(); *(LAS u32x4*)(sm + VT + w * 2048 + l * 16) = pack8(av, 0); SCHEDB(); }
        if constexpr (i == 5) { SCHEDB(); *(LAS u32x4*)(sm + VT + w * 2048 + 1024 + l * 16) = pack8(av, 8); SCHEDB(); }
        if constexpr (i == 6) { SCHEDB(); *(LAS u32x4*)(tb + 2048) = lo; SCHEDB(); }
      });
      *(LAS u32x4*)(tb + 2064) = hi;
    }
    STAMP(7);
    waitv<PFW>();
    BAR();
    STAMP(8);
    lptr ta = sm + PT + w * 1024 + (16 * hh + 4 * blk + q4) * 32 + pp * 8;
    lptr vb = sm + VT + hh * 1024 + (16 * (w >> 1) + i16 + 32 * blk) * 16;
    const u32 one = 0x3F803F80u;
    f32x4 kv = {}, kz = {};
#pragma unroll
    for (int s4 = 0; s4 < 4; ++s4) {
      const u32x4 a = trd2(ta + s4 * 4096, ta + s4 * 4096 + 256);
      kv = mfma16(a, lds128(vb + s4 * 2048), kv);
      kz = mfma16(a, (u32x4){one, one, one, one}, kz);
    }
    const u32 pbase = WS_KVP + (j * 16 + p) * KVS;
    st64c(rs, pbase + w * 512 + l * 8, (u32x2){pk2(kv[0], kv[1]), pk2(kv[2], kv[3])});
    st64c(rs, i16 == 0 ? pbase + 2048 + w * 32 + g4 * 8 : OOB, (u32x2){pk2(kz[0], kz[1]), pk2(kz[2], kz[3])});
    MEMB(); SCHEDB();
  }
  STAMP(9);
  f32x16 aq = {};
  {
    u32x4 wq[2][4];
    auto ldq = [&](int c) NOPK {
      lptr wsl = sm + XR + ((c < 4) ? 32768 : 0) + (c & 3) * 4096 + wlane;
#pragma unroll
      for (int ks = 0; ks < 4; ++ks) wq[c & 1][ks] = trd2(wsl + ks * 1024, wsl + ks * 1024 + 256);
    };
    ldq(0);
#pragma unroll
    for (int c = 0; c < 8; ++c) {
      if (c < 7) ldq(c + 1);
      SCHEDB();
#pragma unroll
      for (int ks = 0; ks < 4; ++ks) aq = mfma32(wq[c & 1][ks], xf[c * 4 + ks], aq);
      SCHEDB();
      if (c == KVFC) { WAITV(0); st64c(rs, l == 0 ? WS_KVF + ((j * 4 + w) * 16 + p) * 128 : OOB, ftag); STAMP(10); }
      if (c == 6) BAR();
    }
  }
  STAMP(11);
  auto issueO = [&](int i) NOPK { dma4(lb + WOB[i] + w * 4096, ov0, ov1, ov2, ov3, so, (128 * w + 32 * i) * 1024); };
  u32x4 pq[2][2], sel[2];
  const u32 kfo = WS_KVF + ((j * 4 + w) * 16 + i16) * 128;
  {
    const f32x16 zero = {};
    issueO(0);
    const f32x16 t0 = mfma32(phif[0], pack8(aq, 0), zero), t1 = mfma32(phif[1], pack8(aq, 8), zero);
    SCHEDB();
    issueO(1);
    const u32 onev = (l & 1) ? 0x3F800000u : 0x00003F80u;
#pragma unroll
    for (int cg = 0; cg < 2; ++cg)
#pragma unroll
      for (int d = 0; d < 4; ++d) sel[cg][d] = ((i16 >> 1) == 4 * cg + d) ? onev : 0u;
    PIN(sel[0]); PIN(sel[1]);
    issueO(2);
    softmax16(t0, pq[0][0], pq[0][1]); PIN(pq[0][0]); PIN(pq[0][1]);
    issueO(3);
    const u32x2 f0 = ld64c(rs, kfo);
    MEMB(); SCHEDB();
    softmax16(t1, pq[1][0], pq[1][1]); PIN(pq[1][0]); PIN(pq[1][1]);
    SCHEDB();
    STAMP(12);
    if (!flag_ok(f0, ftag)) wait_flags(rs, kfo, ftag);
    MEMB();
  }
  STAMP(13);
  {
    const u32 base = WS_KVP + j * 16 * KVS;
    u32x4 v[8];
#pragma unroll
    for (int i = 0; i < 8; ++i) v[i] = ld128c(rs, base + (4 * (i >> 1) + g4) * KVS + w * 512 + (i & 1) * 256 + i16 * 16);
    const u32x2 zv = ld64c(rs, base + (l >> 2) * KVS + 2048 + w * 32 + pp * 8);
    MEMB(); SCHEDB();
    f32x4 a = {};
#pragma unroll
    for (int i = 0; i < 8; ++i) a = mfma16(sel[i & 1], v[i], a);
    *(LAS u32x2*)(sm + KVI + w * 1024 + (pp >> 1) * 512 + (16 + (l >> 2)) * 16 + (pp & 1) * 8) = zv;
    *(LAS u32x2*)(sm + KVI + w * 1024 + hh * 512 + (2 * (l & 7) + blk) * 16 + (i16 >> 3) * 8) = (u32x2){pk2(a[0], a[1]), pk2(a[2], a[3])};
  }
  WAITV(0);
  BAR();
  STAMP(14);
  u32x4 wf[32];
  {
    const f32x16 zero = {};
    lptr ap = sm + KVI + hh * 1024 + r32 * 16;
    f32x16 t[2];
#pragma unroll
    for (int h = 0; h < 2; ++h) { t[h] = mfma32(lds128(ap + h * 2048), pq[h][0], zero); t[h] = mfma32(lds128(ap + h * 2048 + 512), pq[h][1], t[h]); }
#pragma unroll
    for (int h = 0; h < 2; ++h) {
      const float dh = ((t[h][8] + t[h][9]) + (t[h][10] + t[h][11])) + ((t[h][12] + t[h][13]) + (t[h][14] + t[h][15]));
      const auto sw2 = __builtin_amdgcn_permlane32_swap(f2u(dh), f2u(dh), false, false);
      const float r = __builtin_amdgcn_rcpf((u2f(sw2[0]) + u2f(sw2[1])) + 1e-6f);
      st128c(rs, WS_OFR + ((4 * p + w) * 32 + 2 * j + h) * 1024 + l * 16,
             (u32x4){pk2(t[h][0] * r, t[h][1] * r), pk2(t[h][2] * r, t[h][3] * r), pk2(t[h][4] * r, t[h][5] * r), pk2(t[h][6] * r, t[h][7] * r)});
    }
    MEMB(); SCHEDB();
    STAMP(15);
    lptr wb = sm + kh * 8192 + (4 * hh + q4) * 64 + blk * 32 + pp * 8;
    auto ldw = [&](int i) NOPK {
      lptr q = wb + WOB[(i >> 1) & 3] + (i >> 3) * 4096 + (i & 1) * 2048;
      wf[2 * i] = trd2(q, q + 1024); wf[2 * i + 1] = trd2(q + 512, q + 1536);
    };
#pragma unroll
    for (int i = 0; i < 16; ++i) ldw(i);
    WAITV(0);
    st64c(rs, l == 0 ? WS_OF + ((4 * p + w) * 16 + j) * 128 : OOB, ftag);
  }
  BAR();
  STAMP(16);
  if (OPRE) __builtin_amdgcn_s_sleep(OPRE);
  f32x16 acc0 = {}, acc1 = {};
  {
    const u32 obase = WS_OFR + (st * 32 + 16 * kh) * 1024 + l * 16;
    const u32 fo = WS_OF + (st * 16 + 8 * kh + (l & 7)) * 128;
    u32x4 of[16];
    wait_flags(rs, fo, ftag);
    STAMP(18);
#pragma unroll
    for (int i = 0; i < 16; ++i) of[i] = ld128c(rs, obase + i * 1024);
    MEMB(); SCHEDB();
#pragma unroll
    for (int i = 0; i < 16; ++i) { acc0 = mfma32(wf[2 * i], of[i], acc0); acc1 = mfma32(wf[2 * i + 1], of[i], acc1); }
  }
  MEMB(); SCHEDB();
  STAMP(19);
  {
    lptr rp = sm + kh * 17408 + (32 * sh + r32) * 272 + hh * 16;
#pragma unroll
    for (int g = 0; g < 4; ++g) *(LAS f32x4*)(rp + g * 32) = (f32x4){acc0[4 * g], acc0[4 * g + 1], acc0[4 * g + 2], acc0[4 * g + 3]};
#pragma unroll
    for (int g = 0; g < 4; ++g) *(LAS f32x4*)(rp + 128 + g * 32) = (f32x4){acc1[4 * g], acc1[4 * g + 1], acc1[4 * g + 2], acc1[4 * g + 3]};
    BAR();
#pragma unroll
    for (int i = 0; i < 2; ++i) {
      const int row = i * 32 + (tid >> 3), oc = tid & 7;
      lptr sp = sm + row * 272 + oc * 32;
      const f32x4 a = *(LAS f32x4*)sp + *(LAS f32x4*)(sp + 17408), bb = *(LAS f32x4*)(sp + 16) + *(LAS f32x4*)(sp + 17408 + 16);
      __builtin_amdgcn_raw_buffer_store_b128((u32x4){pk2(a[0], a[1]), pk2(a[2], a[3]), pk2(bb[0], bb[1]), pk2(bb[2], bb[3])}, ro, (64 * rt + row) * 2048 + ct * 128 + oc * 16, 0, OAUX);
    }
  }
  st32c(rs, (b == 0 && tid == 0) ? WS_EPOCH : OOB, epoch + 1);
  STAMP(20);
  STAMPS_OUT();
}

DI void prologue_B(u32 lb, const int b, const char* u, const char* Are, const char* Aim, const char* Bre, const char* Bim, const char* Cre, const char* Cim) {
  const int gam = b & 7, idx = b >> 3;
  const int l = threadIdx.x & 63, w = __builtin_amdgcn_readfirstlane(threadIdx.x >> 6);
  const int ar = l >> 3;
  const char* pa = ar < 2 ? (ar ? Aim : Are) : (ar < 4 ? ((ar & 1) ? Bim : Bre) : ((ar & 1) ? Cim : Cre));
  pa += (64 * gam + 2 * idx) * 256 + w * 128 + (l & 7) * 16;
  asm volatile("s_mov_b32 m0, %0\n\ts_nop 0\n\tglobal_load_lds_dwordx4 %1, off" :: "s"(lb + RAW + w * 1024), "v"(pa) : "memory");
  const u32 uo = (l >> 3) * 1024 + (((l & 7) ^ (2 * (l >> 5))) * 16);
  setm0(lb + UT + w * 2048);
  const u32x4 su = mk_srd(u, 2048 * 1024);
  const u32 so = (64 * idx + 16 * w) * 1024 + gam * 128;
  dmao<0>(uo, su, so); dmao<1024>((uo ^ 64) + 7168, su, so);
}

DI void role_B(lptr sm, const int b, const char* ldt, const char* Dsk, char* out, char* ws DBG_ARGS) {
  const int gam = b & 7, idx = b >> 3;
  const int tid = threadIdx.x;
  const int l = tid & 63, w = __builtin_amdgcn_readfirstlane(tid >> 6);
  const int g4 = l >> 4, i16 = l & 15, q4 = i16 >> 2, pp = l & 3, c = w >> 1, hs = w & 1;
  const RSRC rs = __builtin_amdgcn_make_buffer_rsrc(ws, 0, WS_END, 0x00020000);
  const RSRC ro = __builtin_amdgcn_make_buffer_rsrc(out, 0, 4u << 20, 0x00020000);
  STAMPS_DECL; STAMP(1);
  u32 epoch_, ld2_, dk2_;
  asm volatile("s_load_dword %0, %3, %4\n\ts_load_dword %1, %5, %7\n\ts_load_dword %2, %6, %7"
               : "=&s"(epoch_), "=&s"(ld2_), "=&s"(dk2_) : "s"(ws), "s"(WS_EPOCH + 128 * (1 + gam)), "s"(ldt), "s"(Dsk), "s"((64 * gam + 2 * idx) * 2) : "memory");
  lptr kb = sm + KL + c * KST + 32;
  lptr dummy = sm + KL + 2 * KST + l * 16;
  const int beta = i16 < 8 ? i16 : (i16 < 12 ? 240 + 2 * i16 : 128 * (i16 & 14) - 1024 + 4 * (i16 & 1));
  lptr zp = tid < 128 ? sm + UP + (tid >> 3) * UST + 16 + (tid & 7) * 16 : (tid < 132 ? sm + KL + ((tid >> 1) & 1) * KST + (tid & 1) * 16 : dummy);
  *(LAS u32x4*)zp = (u32x4){0, 0, 0, 0};
  u32 epoch, ld2, dk2;
  asm volatile("s_waitcnt vmcnt(2) lgkmcnt(0)\n\ts_mov_b32 %0, %3\n\ts_mov_b32 %1, %4\n\ts_mov_b32 %2, %5"
               : "=&s"(epoch), "=&s"(ld2), "=&s"(dk2) : "s"(epoch_), "s"(ld2_), "s"(dk2_) : "memory");
  STAMP(2);
  const u32 tag = mk_tag(epoch);
  const u32x2 ftag = {tag, mk_tag2(epoch)};
  float a2, brev, ksum; f32x2 cc, m0, m1;
  {
    lptr rp = sm + RAW + w * 1024 + l * 2;
    const float are = bf1(*(LAS u16*)rp), aim = bf1(*(LAS u16*)(rp + 128)), bre = bf1(*(LAS u16*)(rp + 256)), bim = bf1(*(LAS u16*)(rp + 384));
    const float cre = bf1(*(LAS u16*)(rp + 512)), cim = bf1(*(LAS u16*)(rp + 640));
    const float dt = rbf(__builtin_amdgcn_exp2f((c ? bf_hi(ld2) : bf_lo(ld2)) * 1.4426950408889634f));
    cc = cmul((f32x2){cre, cim}, (f32x2){dt * bre, dt * bim});
    a2 = (dt * are) * 1.4426950408889634f; brev = (dt * aim) * 0.15915494309189535f;
    m0 = cexp_s(a2, brev, 128.f); m1 = cexp_s(a2, brev, 1024.f);
    const f32x2 g = cmul(cc, m1);
    ksum = __builtin_fmaf(-g.y, m1.y, g.x * m1.x);
    ksum = addd<0x111, 0xf>(ksum); ksum = addd<0x112, 0xf>(ksum); ksum = addd<0x114, 0xf>(ksum); ksum = addd<0x118, 0xf>(ksum);
    ksum = addd<0x142, 0xa>(ksum); ksum = addd<0x143, 0xc>(ksum);
    PIN(a2); PIN(brev); PIN(cc); PIN(m0); PIN(m1); PIN(ksum);
  }
  MEMB(); SCHEDB();
  STAMP(3);
  {
    f32x4 dA = {}, dB = {}, dC = {}, dD = {};
    const float sA = (float)(8 * i16), sB = (float)beta;
    const u32 ufo = uf_off(gam + 8 * (8 * w + (l & 7)), idx >> 3);
    u32x2 f0;
    auto kgen = [&](auto kk) NOPK {
      constexpr int ks = decltype(kk)::v;
      u32x4 o1, o2, b1, b2;
      auto elem = [&](auto ee) NOPK {
        constexpr int e = decltype(ee)::v, I = 4 * ks + e;
        const f32x2 ea_ = cexp_b<I>(a2, brev, sA), eb = cexp_b<I>(a2, brev, sB);
        const f32x2 v1 = {fnmab<I>(cc.y, ea_.y, mulb<I>(cc.x, ea_.x)), fnmab<I>(cc.y, ea_.x, mulb<I>(cc.x, -ea_.y))};
        const f32x2 v2 = {fmab<I>(m0.y, v1.y, mulb<I>(m0.x, v1.x)), fnmab<I>(m0.y, v1.x, mulb<I>(m0.x, v1.y))};
        const f32x2 w2 = {fnmab<I>(m1.y, eb.y, mulb<I>(m1.x, eb.x)), fmab<I>(m1.y, eb.x, mulb<I>(m1.x, eb.y))};
        o1[e] = pk2(v1.x, v1.y); o2[e] = pk2(v2.x, v2.y); b1[e] = pk2(eb.x, eb.y); b2[e] = pk2(w2.x, w2.y);
      };
      elem(IC<0>{}); elem(IC<1>{}); elem(IC<2>{}); elem(IC<3>{});
      dA = mfma16(b1, o1, dA); dB = mfma16(b1, o2, dB); dC = mfma16(o1, b2, dC); dD = mfma16(o2, b2, dD);
      SCHEDB();
      if constexpr (ks == UPUB - 1) {
        STAMP(4);
        WAITV(0); BAR();
        u32x4 tv[2];
#pragma unroll
        for (int e = 0; e < 2; ++e) {
          lptr a = sm + UT + (32 * (g4 >> 1) + 4 * q4 + 2 * e + (g4 & 1)) * 128 + (((2 * w + (pp >> 1)) ^ (2 * q4)) * 16) + (pp & 1) * 8;
          tv[e] = trd2(a, a + 16 * 128);
        }
#pragma unroll
        for (int e = 0; e < 2; ++e) *(LAS u32x4*)(sm + YS + (16 * w + i16) * 144 + (4 * e + 2 * (g4 & 1) + (g4 >> 1)) * 16) = tv[e];
#pragma unroll
        for (int e = 0; e < 2; ++e)
          st128c(rs, WS_UX + (64 * gam + 16 * w + 8 * e + (l >> 3)) * 4096 + idx * 128 + (l & 7) * 16, lds128(sm + YS + (16 * w + 8 * e + (l >> 3)) * 144 + (l & 7) * 16));
        MEMB(); SCHEDB();
      }
      if constexpr (ks == UPUB) { WAITV(0); st64c(rs, l == 0 ? uf_off(b, w) : OOB, ftag); STAMP(5); }
    };
    kgen(IC<0>{}); kgen(IC<1>{}); kgen(IC<2>{}); kgen(IC<3>{});
    dD[3] = l == 63 ? ksum : dD[3];
    STAMP(6);
    __builtin_amdgcn_s_setprio(BP1);
    f0 = ld64c(rs, ufo);
    const bool first = (i16 == 0) && (hs == 0);
    lptr ea = kb + (8 * i16 + 128 * hs) * 2;
    lptr e1 = ea + (g4 < 2 ? g4 * 8 : (g4 - 1) * 512);
    lptr e2 = g4 < 2 ? dummy : e1 + 8;
    lptr e3 = g4 < 2 ? dummy : (g4 == 2 ? e1 + 12 : e1 - 2);
    lptr e4 = (g4 < 2 || first) ? dummy : (g4 == 2 ? e1 - 2 : e1 - 4);
    lptr e5 = g4 == 3 ? e1 + 512 : dummy + 8;
    const bool rv = (beta & 255) == 0;
    lptr r1 = rv ? kb + (1024 + beta + 128 * hs + 32 * g4) * 2 : dummy + 16;
    lptr r0 = (l == 0 && hs == 0) ? dummy : r1 - 8;
    lptr r2 = (l == 63 && hs == 1) ? kb + 2044 * 2 : r1 + 48;
    PIN(e1); PIN(e2); PIN(e3); PIN(e4); PIN(e5); PIN(r0); PIN(r1); PIN(r2);
    lptr xc = sm + XCH + l * 16;
    *(LAS f32x4*)(xc + w * 1024) = hs ? dA : dB; *(LAS f32x4*)(xc + w * 1024 + 4096) = hs ? dC : dD;
    BAR();
    f32x4 dt_ = (hs ? dB : dA) + *(LAS f32x4*)(xc + (w ^ 1) * 1024);
    const f32x4 dr = (hs ? dD : dC) + *(LAS f32x4*)(xc + (w ^ 1) * 1024 + 4096);
    STAMP(7);
    if (!flag_ok(f0, ftag)) wait_flags(rs, ufo, ftag);
    MEMB();
    u32x4 uv[2];
#pragma unroll
    for (int e = 0; e < 2; ++e) uv[e] = ld128c(rs, WS_UX + (64 * gam + 2 * idx + e) * 4096 + tid * 16);
    MEMB(); SCHEDB();
    STAMP(8);
    dt_[0] += (l == 0 && hs == 0) ? (c ? bf_hi(dk2) : bf_lo(dk2)) : 0.f;
    {
      u32 p00 = pk2(dt_[0], dt_[0]), p01 = pk2(dt_[0], dt_[1]), p11 = pk2(dt_[1], dt_[1]), p12 = pk2(dt_[1], dt_[2]);
      u32 p22 = pk2(dt_[2], dt_[2]), p23 = pk2(dt_[2], dt_[3]), p33 = pk2(dt_[3], dt_[3]);
      PIN(p00); PIN(p01); PIN(p11); PIN(p12); PIN(p22); PIN(p23); PIN(p33);
      const u32 t2 = g4 == 2 ? p12 : p01;
      *(LAS u32x2*)e1 = (u32x2){g4 < 2 ? p01 : p00, g4 < 2 ? p23 : t2};
      *(LAS u32*)e2 = g4 == 2 ? p22 : p11;
      *(LAS u16*)e3 = (u16)(g4 == 2 ? p33 : p00);
      *(LAS u16*)e4 = (u16)p00;
      *(volatile LAS u32*)(e5 - 4) = p22; *(volatile LAS u32x2*)e5 = (u32x2){p22, p23}; *(volatile LAS u32*)(e5 + 8) = p33;
    }
    {
      const u32 p00 = pk2(dr[0], dr[0]), p11 = pk2(dr[1], dr[1]), p22 = pk2(dr[2], dr[2]), p33 = pk2(dr[3], dr[3]);
      *(LAS u32*)r0 = p00;
      *(volatile LAS u32*)(r1 - 4) = p00;
      *(LAS u32x4*)r1 = (u32x4){p00, p00, pk2(dr[0], dr[1]), p11};
      *(LAS u32x4*)(r1 + 16) = (u32x4){p11, p11, p22, p22};
      *(LAS u32x4*)(r1 + 32) = (u32x4){p22, p22, pk2(dr[2], dr[3]), p33};
      *(LAS u32x2*)r2 = (u32x2){p33, p33};
    }
    MEMB(); SCHEDB();
    STAMP(9);
#pragma unroll
    for (int e = 0; e < 2; ++e) {
      lptr q2 = sm + UP + (e * 4 + ((tid >> 1) & 3)) * UST + 128 + (tid >> 3) * 32 + (tid & 1) * 16;
      *(LAS u32x4*)q2 = uv[e];
      *(volatile LAS u32x2*)(q2 + UC2 + 8) = (u32x2){uv[e].x, uv[e].y};
      *(volatile LAS u32x2*)(q2 + UC2 + 16) = (u32x2){uv[e].z, uv[e].w};
    }
  }
  BAR();
  *(LAS f32x4*)(tid < 2 ? sm + CB + tid * CST : dummy) = (f32x4){};
  st32c(rs, (idx == 0 && tid == 0) ? WS_EPOCH + 128 * (1 + gam) : OOB, epoch + 1);
  STAMP(10);
  __builtin_amdgcn_s_setprio(BP1);
  {
    f32x4 acc[4][4];
    lptr ka = kb + (4 * pp - 4 * (8 * g4 + q4) + 112) * 2;
    lptr ub = sm + UP + c * 4 * UST + 128 + (4 * i16 + 8 * g4 - 60) * 2 + ((l & 1) ? 0 : UC2 + 8);
    if (hs == 0) conv_tiles<0, 3, 4, 7>(ka, ub, UST, acc); else conv_tiles<1, 2, 5, 6>(ka, ub, UST, acc);
    STAMP(11);
    __builtin_amdgcn_s_setprio(BP2);
    lptr cb = sm + CB + c * CST + 16;
    f32x4 y[4];
#pragma unroll
    for (int t = 0; t < 4; ++t) {
      const int tau = 2 * t + ((t & 1) ^ hs);
      const f32x4 *z = acc[t];
      *(LAS f32x4*)(cb + (64 * tau + l) * 16) = (f32x4){(z[1][3] + z[2][2]) + z[3][1], z[2][3] + z[3][2], z[3][3], z[3][3]};
    }
#pragma unroll
    for (int t = 0; t < 4; ++t) {
      const f32x4 *z = acc[t];
      y[t] = (f32x4){z[0][0], z[0][1] + z[1][0], (z[0][2] + z[1][1]) + z[2][0], (z[0][3] + z[1][2]) + (z[2][1] + z[3][0])};
      PIN(y[t]);
    }
    BAR();
    STAMP(12);
    const int src = g4 > 0 ? l - 16 : (i16 > 0 ? 47 + i16 : -1);
    u32x2 yp[4];
#pragma unroll
    for (int t = 0; t < 4; ++t) {
      const int tau = 2 * t + ((t & 1) ^ hs);
      const f32x4 r = *(LAS f32x4*)(cb + (64 * tau + src) * 16);
      yp[t] = (u32x2){pk2(y[t][0] + r[0], y[t][1] + r[1]), pk2(y[t][2] + r[2], y[t][3])};
    }
#pragma unroll
    for (int t = 0; t < 4; t += 2) {
      const auto s0 = __builtin_amdgcn_permlane16_swap(yp[t].x, yp[t + 1].x, false, false);
      const auto s1 = __builtin_amdgcn_permlane16_swap(yp[t].y, yp[t + 1].y, false, false);
      const int tau = (g4 & 1) ? 2 * t + 3 - hs : 2 * t + hs;
      st128c(rs, WS_YT + (64 * gam + 2 * idx + c) * 4096 + tau * 512 + q4 * 128 + (g4 >> 1) * 64 + pp * 16, (u32x4){s0[0], s1[0], s0[1], s1[1]});
    }
  }
  MEMB(); SCHEDB();
  STAMP(13);
  WAITV(0);
  st64c(rs, l == 0 ? WS_YF + ((gam * 4 + w) * 32 + idx) * 128 : OOB, ftag);
  STAMP(14);
  __builtin_amdgcn_s_sleep(YPRE);
  {
    const int tl = idx >> 2, hsn = ((tl >> 1) ^ tl) & 1;
    wait_flags(rs, WS_YF + ((gam * 4 + 2 * ((l >> 3) & 1) + hsn) * 32 + 8 * w + (l & 7)) * 128, ftag);
  }
  STAMP(15);
  {
    u32x4 v[2];
#pragma unroll
    for (int e = 0; e < 2; ++e) v[e] = ld128c(rs, WS_YT + (64 * gam + 16 * w + 8 * e + (l >> 3)) * 4096 + idx * 128 + (l & 7) * 16);
#pragma unroll
    for (int e = 0; e < 2; ++e) *(LAS u32x4*)(sm + YS + (16 * w + 8 * e + (l >> 3)) * 144 + (2 * pp + ((l >> 2) & 1)) * 16) = v[e];
  }
  BAR();
#pragma unroll
  for (int e = 0; e < 2; ++e) {
    lptr a = sm + YS + (8 * (4 * e + g4) + q4) * 144 + w * 32 + pp * 8;
    *(LAS u32x4*)(sm + UT + (16 * w + i16) * 144 + (4 * e + g4) * 16) = trd2(a, a + 4 * 144);
  }
#pragma unroll
  for (int e = 0; e < 2; ++e)
    __builtin_amdgcn_raw_buffer_store_b128(lds128(sm + UT + (16 * w + 8 * e + (l >> 3)) * 144 + (l & 7) * 16), ro, (64 * idx + 16 * w + 8 * e + (l >> 3)) * 2048 + 1024 + gam * 128 + (l & 7) * 16, 0, OAUX);
  STAMP(16);
  STAMPS_OUT();
}

#define RUN_A 1
#define RUN_B 1
__global__ __launch_bounds__(256, 2) NOPK void fused_kernel(const char* x, const char* W, const char* wphi, const char* wo, const char* u,
                                                            const char* Are, const char* Aim, const char* Bre, const char* Bim, const char* Cre, const char* Cim,
                                                            const char* ldt, const char* Dsk, char* out, char* ws) {
  extern __shared__ __attribute__((aligned(16))) char smem[];
  lptr sm = (lptr)smem;
  asm volatile("" :: "s"(x), "s"(W), "s"(wphi), "s"(wo), "s"(u), "s"(Are), "s"(Aim), "s"(Bre), "s"(Bim), "s"(Cre), "s"(Cim), "s"(ldt), "s"(Dsk), "s"(out), "s"(ws));
  const int b = blockIdx.x & 255;
  u32 isA = blockIdx.x >> 8;
  __builtin_amdgcn_s_setprio(3);
  if (!isA) prologue_B((u32)(size_t)sm, b, u, Are, Aim, Bre, Bim, Cre, Cim);
  asm volatile("" : "+s"(isA));
  if (__builtin_expect(isA, 1)) { __builtin_amdgcn_s_setprio(APRIO); role_A(sm, b, x, W, wphi, wo, out, ws DBG_PASS); }
  else { __builtin_amdgcn_s_setprio(BP0); role_B(sm, b, ldt, Dsk, out, ws DBG_PASS); }
}

extern "C" void kernel_launch(void* const* d_in, const int* in_sizes, int n_in, void* d_out, int out_size, void* d_ws, size_t ws_size,
                              hipStream_t stream) {
  (void)hipFuncSetAttribute((const void*)fused_kernel, hipFuncAttributeMaxDynamicSharedMemorySize, LDS_BYTES);
  fused_kernel<<<512, 256, LDS_BYTES, stream>>>((const char*)d_in[0], (const char*)d_in[1], (const char*)d_in[2], (const char*)d_in[3], (const char*)d_in[4],
                                                (const char*)d_in[5], (const char*)d_in[6], (const char*)d_in[7], (const char*)d_in[8], (const char*)d_in[9], (const char*)d_in[10],
                                                (const char*)d_in[11], (const char*)d_in[12], (char*)d_out, (char*)d_ws);
}
```

```cpp
#include <hip/hip_runtime.h>
#include <hip/hip_bf16.h>

typedef unsigned int u32;
typedef unsigned long long u64;
typedef unsigned short u16;
typedef short s16x4 __attribute__((ext_vector_type(4)));
typedef __bf16 bf16x2 __attribute__((ext_vector_type(2)));
typedef __bf16 bf16x8 __attribute__((ext_vector_type(8)));
typedef float f32x2 __attribute__((ext_vector_type(2)));
typedef float f32x4 __attribute__((ext_vector_type(4)));
typedef float f32x16 __attribute__((ext_vector_type(16)));
typedef u32 u32x2 __attribute__((ext_vector_type(2)));
typedef u32 u32x4 __attribute__((ext_vector_type(4)));
#define LAS __attribute__((address_space(3)))
#if defined(__HIP_DEVICE_COMPILE__)
#define NOPK __attribute__((target("no-packed-fp32-ops")))
#else
#define NOPK
#endif
#define DI __device__ __forceinline__ NOPK
typedef LAS char* lptr;

constexpr u32 WS_EPOCH = 0;
constexpr u32 WS_OF = 4096;
constexpr u32 WS_UF = WS_OF + 1024 * 128;
constexpr u32 WS_YF = WS_UF + 1024 * 128;
constexpr u32 WS_KVF = WS_YF + 1024 * 128;
constexpr u32 WS_KVP = 1u << 20;
constexpr u32 KVS = 2176;
constexpr u32 WS_OFR = 2u << 20;
constexpr u32 WS_UX = 4u << 20;
constexpr u32 WS_YT = WS_UX;
constexpr u32 WS_END = 6u << 20;
constexpr u32 OOB = 0x7FFFFFF0u;
constexpr int LDS_BYTES = 81920;
constexpr u32 SPIN_CAP = 1u << 16;
constexpr int PSLEEP = 16, PRE_SLEEP = 20, UPRE = 16;
constexpr u32 CODE_LINES = 168;
constexpr int OAUX = 17;

#define BAR() asm volatile("s_waitcnt lgkmcnt(0)\n\ts_barrier" ::: "memory")
#define WAITV(n) asm volatile("s_waitcnt vmcnt(" #n ")" ::: "memory")
#define MEMB() asm volatile("" ::: "memory")
#define SCHEDB() __builtin_amdgcn_sched_barrier(0)

DI float u2f(u32 v) { return __builtin_bit_cast(float, v); }
DI u32 f2u(float v) { return __builtin_bit_cast(u32, v); }
DI bool all_lanes(bool c) { return __builtin_amdgcn_ballot_w64(!c) == 0; }
DI u32 pk2(float a, float b) { f32x2 v = {a, b}; return __builtin_bit_cast(u32, __builtin_convertvector(v, bf16x2)); }
DI float bf_lo(u32 v) { return u2f(v << 16); }
DI float bf_hi(u32 v) { return u2f(v & 0xffff0000u); }
DI float bf1(u16 v) { return u2f((u32)v << 16); }
DI float rbf(float a) { return bf_lo(pk2(a, a)); }
DI u32x2 trd(lptr p) { return __builtin_bit_cast(u32x2, __builtin_amdgcn_ds_read_tr16_b64_v4i16((LAS s16x4*)p)); }
DI u32x4 trd2(lptr p0, lptr p1) { u32x2 a = trd(p0), b = trd(p1); return (u32x4){a.x, a.y, b.x, b.y}; }
DI u32x4 lds128(lptr p) { return *(LAS u32x4*)p; }
DI f32x16 mfma32(u32x4 a, u32x4 b, f32x16 c) { return __builtin_amdgcn_mfma_f32_32x32x16_bf16(__builtin_bit_cast(bf16x8, a), __builtin_bit_cast(bf16x8, b), c, 0, 0, 0); }
DI f32x4 mfma16(u32x4 a, u32x4 b, f32x4 c) { return __builtin_amdgcn_mfma_f32_16x16x32_bf16(__builtin_bit_cast(bf16x8, a), __builtin_bit_cast(bf16x8, b), c, 0, 0, 0); }
DI u32x4 mk_srd(const void* p, u32 bytes) { const u64 a = (u64)p; return (u32x4){(u32)a, (u32)(a >> 32) & 0xffffu, bytes, 0x00020000u}; }
DI void dma1(u32 lds, u32 v0, u32x4 srd, u32 soff) {
  asm volatile("s_mov_b32 m0, %0\n\ts_nop 0\n\tbuffer_load_dwordx4 %1, %2, %3 offen lds" :: "s"(lds), "v"(v0), "s"(srd), "s"(soff) : "memory");
}
DI void dma4(u32 lds, u32 v0, u32 v1, u32 v2, u32 v3, u32x4 srd, u32 soff) {
  asm volatile("s_mov_b32 m0, %0\n\ts_nop 0\n\tbuffer_load_dwordx4 %1, %5, %6 offen lds\n\tbuffer_load_dwordx4 %2, %5, %6 offen offset:1024 lds\n\t"
               "buffer_load_dwordx4 %3, %5, %6 offen offset:2048 lds\n\tbuffer_load_dwordx4 %4, %5, %6 offen offset:3072 lds"
               :: "s"(lds), "v"(v0), "v"(v1), "v"(v2), "v"(v3), "s"(srd), "s"(soff) : "memory");
}
DI void setm0(u32 lds) { asm volatile("s_mov_b32 m0, %0\n\ts_nop 0" :: "s"(lds) : "memory"); }
template <int OFF> DI void dmao_nt(u32 v, u32x4 srd, u32 soff) {
  asm volatile("buffer_load_dwordx4 %0, %1, %2 offen offset:%3 nt lds" :: "v"(v), "s"(srd), "s"(soff), "i"(OFF) : "memory");
}
template <int OFF> DI void dmao(u32 v, u32x4 srd, u32 soff) {
  asm volatile("buffer_load_dwordx4 %0, %1, %2 offen offset:%3 lds" :: "v"(v), "s"(srd), "s"(soff), "i"(OFF) : "memory");
}
#define PIN(x) asm volatile("" : "+v"(x))
DI u32 mk_tag(u32 e) { const u32 t = (e + 0x1B4E0193u) * 0x2F4A7C15u; return (t & 0x3FFFu) | ((t & 0x1FFFC000u) << 1) | ((t & 0x20000000u) << 2); }
DI u32 mk_tag2(u32 e) { return (e ^ 0x5BD1E995u) * 0x9E3779B1u; }
#define RSRC __amdgpu_buffer_rsrc_t
DI void st128c(RSRC r, u32 off, u32x4 v) { __builtin_amdgcn_raw_buffer_store_b128(v, r, off, 0, 16); }
DI void st64c(RSRC r, u32 off, u32x2 v) { __builtin_amdgcn_raw_buffer_store_b64(v, r, off, 0, 16); }
DI void st32c(RSRC r, u32 off, u32 v) { __builtin_amdgcn_raw_buffer_store_b32(v, r, off, 0, 16); }
DI u32x4 ld128c(RSRC r, u32 off) { return __builtin_amdgcn_raw_buffer_load_b128(r, off, 0, 16); }
DI u32x2 ld64c(RSRC r, u32 off) { return __builtin_amdgcn_raw_buffer_load_b64(r, off, 0, 16); }
DI void put_flag(RSRC r, u32 off, u32x2 tg, int l) {
  st128c(r, l < 8 ? off + l * 16 : OOB, (u32x4){tg.x, tg.y, tg.x, tg.y});
}
DI bool flag_ok(u32x2 f, u32x2 tg) { return all_lanes(f.x == tg.x && f.y == tg.y); }
DI void wait_flags(RSRC r, u32 off, u32x2 tg) {
#pragma unroll 1
  for (u32 spins = 0; spins < SPIN_CAP; ++spins) {
    MEMB();
    if (flag_ok(ld64c(r, off), tg)) break;
    __builtin_amdgcn_s_sleep(PSLEEP);
  }
  MEMB();
}
DI u32 uf_off(int blk, int w) { return WS_UF + (((blk & 7) * 4 + w) * 32 + (blk >> 3)) * 128; }

template <int N> struct IC { static constexpr int v = N; };

DI float max3f(float a, float b, float c) { float r; asm("v_max3_f32 %0, %1, %2, %3" : "=v"(r) : "v"(a), "v"(b), "v"(c)); return r; }
template <class H> DI void softmax16h(const f32x16& p, u32x4& lo, u32x4& hi, H hook) {
  const float m0 = __builtin_fmaxf(p[0], p[1]);
  const float m1 = __builtin_fmaxf(p[14], p[15]);
  float ma = max3f(m0, p[2], p[3]), mb_ = max3f(m1, p[4], p[5]);
  ma = max3f(ma, p[6], p[7]); mb_ = max3f(mb_, p[8], p[9]);
  ma = max3f(ma, p[10], p[11]); mb_ = max3f(mb_, p[12], p[13]);
  const float L2E = 1.4426950408889634f;
  const float mb = max3f(ma, mb_, mb_) * -L2E;
  hook(IC<0>{});
  float e[16];
#pragma unroll
  for (int i = 0; i < 4; ++i) e[i] = __builtin_amdgcn_exp2f(__builtin_fmaf(p[i], L2E, mb));
  hook(IC<1>{});
#pragma unroll
  for (int i = 4; i < 8; ++i) e[i] = __builtin_amdgcn_exp2f(__builtin_fmaf(p[i], L2E, mb));
  hook(IC<2>{});
#pragma unroll
  for (int i = 8; i < 12; ++i) e[i] = __builtin_amdgcn_exp2f(__builtin_fmaf(p[i], L2E, mb));
  hook(IC<3>{});
#pragma unroll
  for (int i = 12; i < 16; ++i) e[i] = __builtin_amdgcn_exp2f(__builtin_fmaf(p[i], L2E, mb));
  hook(IC<4>{});
  const float s0 = ((e[0] + e[1]) + (e[2] + e[3])) + ((e[4] + e[5]) + (e[6] + e[7]));
  const float s1 = ((e[8] + e[9]) + (e[10] + e[11])) + ((e[12] + e[13]) + (e[14] + e[15]));
  const float r = __builtin_amdgcn_rcpf(s0 + s1);
  hook(IC<5>{});
  lo = (u32x4){pk2(e[0] * r, e[1] * r), pk2(e[2] * r, e[3] * r), pk2(e[4] * r, e[5] * r), pk2(e[6] * r, e[7] * r)};
  hook(IC<6>{});
  hi = (u32x4){pk2(e[8] * r, e[9] * r), pk2(e[10] * r, e[11] * r), pk2(e[12] * r, e[13] * r), pk2(e[14] * r, e[15] * r)};
  hook(IC<7>{});
}
DI void softmax16(const f32x16& p, u32x4& lo, u32x4& hi) { softmax16h(p, lo, hi, [](auto) NOPK {}); }
DI u32x4 pack8(const f32x16& a, int o) { return (u32x4){pk2(a[o], a[o + 1]), pk2(a[o + 2], a[o + 3]), pk2(a[o + 4], a[o + 5]), pk2(a[o + 6], a[o + 7])}; }

DI f32x2 cmul(f32x2 a, f32x2 b) { return (f32x2){__builtin_fmaf(-a.y, b.y, a.x * b.x), __builtin_fmaf(a.y, b.x, a.x * b.y)}; }
DI f32x2 cexp_s(float a2, float brev, float s) {
  const float m = __builtin_amdgcn_exp2f(a2 * s);
  const float th = __builtin_amdgcn_fractf(brev * s);
  return (f32x2){m * __builtin_amdgcn_cosf(th), m * __builtin_amdgcn_sinf(th)};
}
template <int I> DI float bcast(float q) {
  return __builtin_bit_cast(float, __builtin_amdgcn_update_dpp(0, __builtin_bit_cast(int, q), 0x150 + I, 0xf, 0xf, true));
}
template <int I> DI float mulb(float q, float x) { return bcast<I>(q) * x; }
#define BC(n) " row_newbcast:%" #n " row_mask:0xf bank_mask:0xf\n\t"
template <int I> DI void cprod2(f32x2 cc, f32x2 m0, f32x2 m1, const f32x2 (&a)[2], const f32x2 (&b)[2], f32x2 (&v1)[2], f32x2 (&v2)[2], f32x2 (&w2)[2]) {
  float o[12];
  asm("s_nop 1\n\t"
      "v_mul_f32_dpp %0, %20, %12" BC(26) "v_mul_f32_dpp %1, -%20, %13" BC(26) "v_mul_f32_dpp %2, %20, %14" BC(27) "v_mul_f32_dpp %3, -%20, %15" BC(27)
      "v_mul_f32_dpp %4, %24, %16" BC(26) "v_mul_f32_dpp %5, %24, %17" BC(26) "v_mul_f32_dpp %6, %24, %18" BC(27) "v_mul_f32_dpp %7, %24, %19" BC(27)
      "v_fmac_f32_dpp %0, -%21, %13" BC(26) "v_fmac_f32_dpp %1, -%21, %12" BC(26) "v_fmac_f32_dpp %2, -%21, %15" BC(27) "v_fmac_f32_dpp %3, -%21, %14" BC(27)
      "v_fmac_f32_dpp %4, -%25, %17" BC(26) "v_fmac_f32_dpp %5, %25, %16" BC(26) "v_fmac_f32_dpp %6, -%25, %19" BC(27) "v_fmac_f32_dpp %7, %25, %18" BC(27)
      "v_mul_f32_dpp %8, %22, %0" BC(26) "v_mul_f32_dpp %9, %22, %1" BC(26) "v_mul_f32_dpp %10, %22, %2" BC(27) "v_mul_f32_dpp %11, %22, %3" BC(27)
      "v_fmac_f32_dpp %8, %23, %1" BC(26) "v_fmac_f32_dpp %9, -%23, %0" BC(26) "v_fmac_f32_dpp %10, %23, %3" BC(27) "v_fmac_f32_dpp %11, -%23, %2" BC(27)
      : "=&v"(o[0]), "=&v"(o[1]), "=&v"(o[2]), "=&v"(o[3]), "=&v"(o[4]), "=&v"(o[5]), "=&v"(o[6]), "=&v"(o[7]), "=&v"(o[8]), "=&v"(o[9]), "=&v"(o[10]), "=&v"(o[11])
      : "v"(a[0].x), "v"(a[0].y), "v"(a[1].x), "v"(a[1].y), "v"(b[0].x), "v"(b[0].y), "v"(b[1].x), "v"(b[1].y),
        "v"(cc.x), "v"(cc.y), "v"(m0.x), "v"(m0.y), "v"(m1.x), "v"(m1.y), "i"(I), "i"(I + 1));
  v1[0] = (f32x2){o[0], o[1]}; v1[1] = (f32x2){o[2], o[3]}; w2[0] = (f32x2){o[4], o[5]}; w2[1] = (f32x2){o[6], o[7]};
  v2[0] = (f32x2){o[8], o[9]}; v2[1] = (f32x2){o[10], o[11]};
}
template <int I> DI f32x2 cexp_b(float a2, float brev, float s) {
  const float m = __builtin_amdgcn_exp2f(mulb<I>(a2, s));
  const float th = __builtin_amdgcn_fractf(mulb<I>(brev, s));
  return (f32x2){m * __builtin_amdgcn_cosf(th), m * __builtin_amdgcn_sinf(th)};
}
template <int CTRL, int RM> DI float addd(float v) {
  return v + __builtin_bit_cast(float, __builtin_amdgcn_update_dpp(0, __builtin_bit_cast(int, v), CTRL, RM, 0xf, true));
}

template <int T0, int T1, int T2, int T3>
DI void conv_tiles(lptr ka, lptr ub, int ust, f32x4 (&acc)[4][4]) {
  constexpr int T[4] = {T0, T1, T2, T3};
  constexpr int NB = 2 * T3 + 2, PF = 8;
  u32x4 af[NB], bfr[4 * NB];
  bool have[NB] = {};
  auto lda = [&](int m) NOPK { af[m] = trd2(ka + m * 256, ka + m * 256 - 32); };
  auto ldb = [&](int n) NOPK {
    lptr q = ub + (n / NB) * ust + (n % NB) * 64;
    bfr[n] = lds128(q);
  };
  auto need = [&](int n) NOPK {
    ldb(n);
    if (n < NB) {
#pragma unroll
      for (int t = 0; t < 4; ++t) { const int m = 2 * T[t] + 1 - n; if (m >= 0 && !have[m]) { lda(m); have[m] = true; } }
    }
  };
  need(0); need(1); need(2); need(3);
#pragma unroll
  for (int n = 0; n < 4 * NB; n += 2) {
    const int rho = n / NB, be = n % NB;
    asm volatile("" :: "v"(bfr[n + 1]));
    SCHEDB();
    int cnt = 0;
#pragma unroll
    for (int h2 = 0; h2 < 2; ++h2) {
#pragma unroll
      for (int t = 3; t >= 0; --t) {
        const int m = 2 * T[t] + 1 - (be + h2);
        if (m >= 0) {
          acc[t][rho] = mfma16(af[m], bfr[n + h2], (be + h2) == 0 ? (f32x4){} : acc[t][rho]);
          if (cnt < 2) {
            if (n < PF - 4) { need(2 * n + 4 + 2 * cnt); need(2 * n + 5 + 2 * cnt); }
            else if (n + PF + cnt < 4 * NB) need(n + PF + cnt);
            SCHEDB();
          }
          ++cnt;
        }
      }
    }
    SCHEDB();
  }
}

constexpr int XR = 0, KR = 49152, VR = 61440, PHI = 77824, KVI = KR + 8192;
constexpr int CB = 0, CST = 8208;
constexpr int XCH = 0;
constexpr int YS = 16640;
constexpr int RAW = 26112;
constexpr int UT = 36352;
constexpr int UP = 45568, UST = 1168, UC2 = 9344;
constexpr int KL = 64256, KST = 4448;

struct AAddr { u32 xv0, xv1, xv2, xv3, wv0; };
DI AAddr a_addr(int p, int j, int w, int l) {
  AAddr a;
  a.xv0 = (128 * p + 32 * w + (l >> 3)) * 1024 + ((l & 7) ^ (l >> 4)) * 16;
  a.xv1 = (a.xv0 ^ 64) + 7168; a.xv2 = a.xv0 + 2 * 7168; a.xv3 = a.xv1 + 2 * 7168;
  a.wv0 = (l >> 2) * 3072 + j * 64 + (l & 3) * 16;
  return a;
}
DI void role_A(lptr sm, const int b, u32 epoch_, u32 ld2_, u32 dk2_, const char* x, const char* W, const char* wphi, const char* wo, char* out, char* ws) {
  const int xcd = b & 7, idx = b >> 3;
  const int p = (xcd >> 1) * 4 + (idx >> 3), j = (xcd & 1) * 8 + (idx & 7);
  const int tid = threadIdx.x;
  const int l = tid & 63, w = __builtin_amdgcn_readfirstlane(tid >> 6);
  const int hh = l >> 5, blk = (l >> 4) & 1, i16 = l & 15, g4 = l >> 4, q4 = i16 >> 2, pp = l & 3, r32 = l & 31;
  const u32 lb = (u32)(size_t)sm;
  const int r0 = 2 * (xcd >> 1) + 4 * (xcd & 1) + ((idx + 2 * (idx >> 3)) & 3);
  const u32x4 sx = mk_srd(x, 2048 * 1024), sw = mk_srd(W, 512 * 3072);
  const AAddr ad = a_addr(p, j, w, l);
  auto vslot = [&](int c) NOPK { return c < 7 ? VR + (c & 3) * 4096 : KR + 8192; };
  auto issueW = [&](int dst, int mat, int c) NOPK { dma1(lb + dst + w * 1024, ad.wv0, sw, (((c + r0) & 7) * 64 + 16 * w) * 3072 + mat * 1024); };
#pragma unroll
  for (int c = 0; c < 3; ++c) {
    dma4(lb + XR + c * 16384 + w * 4096, ad.xv0, ad.xv1, ad.xv2, ad.xv3, sx, ((c + r0) & 7) * 128);
    if (c < 2) dma1(lb + KR + c * 4096 + w * 1024, ad.wv0, sw, (((c + r0) & 7) * 64 + 16 * w) * 3072 + 1024);
  }
  dma1(lb + PHI, l * 16, mk_srd(wphi, 32 * 512), j * 1024);
  issueW(vslot(0), 2, 0);
  {
    asm volatile("s_mov_b32 m0, %0\n\ts_nop 0\n\tbuffer_load_dword %1, %2, %3 offen lds" :: "s"(lb + PHI + 1024), "v"(ad.wv0 & ~63u), "s"(sw), "s"(((r0 & 7) * 64 + 16 * w) * 3072) : "memory");
  }
  const RSRC rs = __builtin_amdgcn_make_buffer_rsrc(ws, 0, WS_END, 0x00020000);
  const RSRC ro = __builtin_amdgcn_make_buffer_rsrc(out, 0, 4u << 20, 0x00020000);
  const u32 wv1 = ad.wv0 + 48128, wv2 = ad.wv0 + 2 * 48128, wv3 = ad.wv0 + 3 * 48128;
  const int rt = xcd * 4 + (idx >> 3), ct = idx & 7;
  const int sh = w & 1, kh = w >> 1, st = 2 * rt + sh;

  const int wlane = (8 * hh + q4) * 64 + blk * 32 + pp * 8;
  const int xsw = (r32 >> 1) & 7;
  u32 epoch;
  u64 pft;
  lptr vbase = sm + KR + wlane; PIN(vbase);
  u32x4 xf[32], phif[2];
  f32x16 ak = {}, av = {};
#pragma unroll
  for (int c = 0; c < 8; ++c) {
    if (c < 2) WAITV(12); else if (c == 2) WAITV(10); else if (c < 6) WAITV(11); else WAITV(7);
    BAR();
    if (c == 0) { asm volatile("s_mov_b32 %0, %1" : "=s"(epoch) : "s"(epoch_), "s"(ld2_), "s"(dk2_)); }
    lptr xs = sm + XR + (c % 3) * 16384 + w * 4096 + r32 * 128;
    lptr wsl = vbase + (c % 3) * 4096;
    lptr vsl = vbase + (vslot(c - 3) - KR);
    u32x4 wk[4], wv[4];
#pragma unroll
    for (int ks = 0; ks < 4; ++ks) { xf[c * 4 + ks] = lds128(xs + (((2 * ks + hh) ^ xsw) * 16)); wk[ks] = trd2(wsl + ks * 1024, wsl + ks * 1024 + 256); }
    if (c + 2 < 8) issueW(KR + ((c + 2) % 3) * 4096, 1, c + 2);
    if (c == 6) { issueW(vslot(6), 2, 6); issueW(vslot(7), 2, 7); }
    SCHEDB();
#pragma unroll
    for (int ks = 0; ks < 4; ++ks) {
      ak = mfma32(wk[ks], xf[c * 4 + ks], ak);
      if (c >= 3 && c < 7) wv[ks] = trd2(vsl + ks * 1024, vsl + ks * 1024 + 256);
      SCHEDB();
    }
    if (c < 7) {
      const bool isx = c + 3 < 8, isq = c >= 6;
      const u32 so_ = isx ? (u32)(((c + 3 + r0) & 7) * 128) : (u32)(((4 * (c - 6) + w + r0) & 7) * 64 * 3072);
      if (isx || isq) setm0(lb + XR + ((isx ? c : c - 1) % 3) * 16384 + w * 4096);
      if (c >= 3) av = mfma32(xf[(c - 3) * 4], wv[0], av);
      if (isx) dmao<0>(ad.xv0, sx, so_); else if (isq) dmao<0>(ad.wv0, sw, so_);
      SCHEDB();
      if (c >= 3) av = mfma32(xf[(c - 3) * 4 + 1], wv[1], av);
      if (isx) dmao<1024>(ad.xv1, sx, so_); else if (isq) dmao<1024>(wv1, sw, so_);
      SCHEDB();
      if (c >= 3) av = mfma32(xf[(c - 3) * 4 + 2], wv[2], av);
      if (isx) dmao<2048>(ad.xv2, sx, so_); else if (isq) dmao<2048>(wv2, sw, so_);
      SCHEDB();
      if (c >= 3) av = mfma32(xf[(c - 3) * 4 + 3], wv[3], av);
      if (isx) dmao<3072>(ad.xv3, sx, so_); else if (isq) dmao<3072>(wv3, sw, so_);
      if (c > 0 && c < 6) issueW(vslot(c), 2, c);
    }
    if (c == 1) {
      const u64 o0 = (u64)wo + (idx * 128 + w * 32 + r32) * 128, o1 = (u64)out + (u32)(blk ? 64 * idx : 64 * rt) * 2048u;
      pft = hh ? o1 : o0;
      PIN(pft);
    }
    if (c == 2) {
      const u32 sg = ((l >> 2) & 1) ? 0x80008000u : 0u;
      lptr pb = sm + PHI + (4 * hh + q4) * 32 + (8 * blk + 4 * (pp >> 1)) * 2;
#pragma unroll
      for (int h = 0; h < 2; ++h) { u32x4 v = trd2(pb + h * 512, pb + h * 512 + 256); phif[h] = (u32x4){v.x ^ sg, v.y ^ sg, v.z ^ sg, v.w ^ sg}; }
    }
    SCHEDB();
  }
  const u32 tag = mk_tag(epoch);
  const u32x2 ftag = {tag, mk_tag2(epoch)};
  const u32x4 so = mk_srd(wo, 512 * 1024);
  const u32 ov0 = ((l >> 2) & 7) * 1024 + ct * 128 + hh * 64 + pp * 16;
  const u32 ov1 = ov0 + 7168, ov2 = ov0 + 2 * 7168, ov3 = ov0 + 3 * 7168;
  constexpr int WOB[4] = {VR, 16384, 32768, 0};
  {
    constexpr int PT = XR + 16384;
    const f32x16 zero = {};
    f32x16 pk[2];
    pk[0] = mfma32(phif[0], pack8(ak, 0), zero); pk[1] = mfma32(phif[1], pack8(ak, 8), zero);
    lptr tb = sm + PT + w * 4096 + l * 32;
    lptr vt_w = sm + ((w & 2) ? VR + 3 * 4096 : KR) + (w & 1) * 2048 + l * 16;
    {
      u32x4 vf[16];
      auto ldv = [&](int i) NOPK { lptr q = vbase + (vslot(4 + (i >> 2)) - KR) + (i & 3) * 1024; vf[i] = trd2(q, q + 256); };
#pragma unroll
      for (int i = 0; i < 4; ++i) ldv(i);
      const u32 sq = ((4 + w + r0) & 7) * 64 * 3072;
      setm0(lb + XR + w * 4096);
      SCHEDB();
      u32x4 lo, hi;
      softmax16h(pk[0], lo, hi, [&](auto ii) NOPK {
        constexpr int i = decltype(ii)::v;
        SCHEDB();
        av = mfma32(xf[16 + i], vf[i], av);
        if constexpr (i == 0) { WAITV(4); BAR(); ldv(4); }
        ldv(i + 5);
        if constexpr (i == 0) dmao<0>(ad.wv0, sw, sq);
        if constexpr (i == 2) dmao<1024>(wv1, sw, sq);
        if constexpr (i == 4) dmao<2048>(wv2, sw, sq);
        if constexpr (i == 6) dmao<3072>(wv3, sw, sq);
        SCHEDB();
      });
      *(LAS u32x4*)tb = lo; *(LAS u32x4*)(tb + 16) = hi;
      softmax16h(pk[1], lo, hi, [&](auto ii) NOPK {
        constexpr int i = decltype(ii)::v;
        if constexpr (i < 4) {
          SCHEDB();
          av = mfma32(xf[24 + 2 * i], vf[8 + 2 * i], av);
          if constexpr (i < 2) ldv(13 + 2 * i);
          if constexpr (i == 0) ldv(14);
          av = mfma32(xf[25 + 2 * i], vf[9 + 2 * i], av);
          if constexpr (i == 2)
            asm volatile("s_mov_b32 m0, %0\n\ts_nop 0\n\tglobal_load_lds_dword %1, off" :: "s"(lb + PHI + 1024), "v"(pft) : "memory");
          SCHEDB();
        }
        if constexpr (i == 5) { SCHEDB(); *(LAS u32x4*)(vt_w) = pack8(av, 0); *(LAS u32x4*)(vt_w + 1024) = pack8(av, 8); SCHEDB(); }
        if constexpr (i == 6) { SCHEDB(); *(LAS u32x4*)(tb + 2048) = lo; SCHEDB(); }
      });
      *(LAS u32x4*)(tb + 2064) = hi;
    }
    lptr ta = sm + PT + w * 1024 + (16 * hh + 4 * blk + q4) * 32 + pp * 8;
    lptr vb = sm + KR + hh * 1024 + (16 * (w >> 1) + i16 + 32 * blk) * 16;
    const u32 pbase = WS_KVP + (j * 16 + p) * KVS;
    u32 so0 = pbase + w * 512 + l * 8, so1 = i16 == 0 ? pbase + 2048 + w * 32 + g4 * 8 : OOB;
    u32x4 ones = {0x3F803F80u, 0x3F803F80u, 0x3F803F80u, 0x3F803F80u};
    PIN(ta); PIN(vb); PIN(so0); PIN(so1); PIN(ones);
    WAITV(1);
    BAR();
    u32x4 fa[4], fb[4];
#pragma unroll
    for (int s4 = 0; s4 < 4; ++s4) { fa[s4] = trd2(ta + s4 * 4096, ta + s4 * 4096 + 256); fb[s4] = lds128(vb + (s4 & 1) * 2048 + (s4 >> 1) * (VR + 3 * 4096 - KR)); }
    SCHEDB();
    f32x4 kv = {}, kz = {};
#pragma unroll
    for (int s4 = 0; s4 < 4; ++s4) { kv = mfma16(fa[s4], fb[s4], kv); kz = mfma16(fa[s4], ones, kz); }
    st64c(rs, so0, (u32x2){pk2(kv[0], kv[1]), pk2(kv[2], kv[3])});
    st64c(rs, so1, (u32x2){pk2(kz[0], kz[1]), pk2(kz[2], kz[3])});
    MEMB(); SCHEDB();
  }
  f32x16 aq = {};
  {
    u32x4 wq[2][4];
    auto ldq = [&](int c) NOPK {
      lptr wsl = sm + XR + ((c < 4) ? 32768 : 0) + (c & 3) * 4096 + wlane;
#pragma unroll
      for (int ks = 0; ks < 4; ++ks) wq[c & 1][ks] = trd2(wsl + ks * 1024, wsl + ks * 1024 + 256);
    };
    ldq(0);
#pragma unroll
    for (int c = 0; c < 8; ++c) {
      if (c < 7) ldq(c + 1);
      SCHEDB();
#pragma unroll
      for (int ks = 0; ks < 4; ++ks) aq = mfma32(wq[c & 1][ks], xf[c * 4 + ks], aq);
      SCHEDB();
      if (c == 4) { WAITV(0); put_flag(rs, WS_KVF + ((j * 4 + w) * 16 + p) * 128, ftag, l); }
      if (c == 6) BAR();
    }
  }
  auto issueO = [&](int i) NOPK { dma4(lb + WOB[i] + w * 4096, ov0, ov1, ov2, ov3, so, (128 * w + 32 * i) * 1024); };
  u32x4 pq[2][2], sel[2];
  u32 go = g4 * KVS + w * 512 + i16 * 16, zo = (l >> 2) * KVS + 2048 + w * 32 + pp * 8;
  const u32 kfo = WS_KVF + ((j * 4 + w) * 16 + i16) * 128;
  {
    const f32x16 zero = {};
    issueO(0);
    const f32x16 t0 = mfma32(phif[0], pack8(aq, 0), zero), t1 = mfma32(phif[1], pack8(aq, 8), zero);
    SCHEDB();
    issueO(1);
    const u32 onev = (l & 1) ? 0x3F800000u : 0x00003F80u;
#pragma unroll
    for (int cg = 0; cg < 2; ++cg)
#pragma unroll
      for (int d = 0; d < 4; ++d) sel[cg][d] = ((i16 >> 1) == 4 * cg + d) ? onev : 0u;
    PIN(sel[0]); PIN(sel[1]);
    issueO(2);
    softmax16(t0, pq[0][0], pq[0][1]); PIN(pq[0][0]); PIN(pq[0][1]);
    issueO(3);
    PIN(go); PIN(zo);
    const u32x2 f0 = ld64c(rs, kfo);
    MEMB(); SCHEDB();
    softmax16(t1, pq[1][0], pq[1][1]); PIN(pq[1][0]); PIN(pq[1][1]);
    SCHEDB();
    if (!flag_ok(f0, ftag)) wait_flags(rs, kfo, ftag);
    MEMB();
  }
  {
    const u32 base = WS_KVP + j * 16 * KVS;
    u32x4 v[8];
#pragma unroll
    for (int i = 0; i < 8; ++i) v[i] = __builtin_amdgcn_raw_buffer_load_b128(rs, go + (i & 1) * 256, base + 4 * (i >> 1) * KVS, 16);
    const u32x2 zv = __builtin_amdgcn_raw_buffer_load_b64(rs, zo, base, 16);
    MEMB(); SCHEDB();
    f32x4 a = {};
#pragma unroll
    for (int i = 0; i < 8; ++i) a = mfma16(sel[i & 1], v[i], a);
    *(LAS u32x2*)(sm + KVI + w * 1024 + (pp >> 1) * 512 + (16 + (l >> 2)) * 16 + (pp & 1) * 8) = zv;
    *(LAS u32x2*)(sm + KVI + w * 1024 + hh * 512 + (2 * (l & 7) + blk) * 16 + (i16 >> 3) * 8) = (u32x2){pk2(a[0], a[1]), pk2(a[2], a[3])};
  }
  WAITV(0);
  BAR();
  u32x4 wf[32];
  {
    const f32x16 zero = {};
    lptr ap = sm + KVI + hh * 1024 + r32 * 16;
    f32x16 t[2];
#pragma unroll
    for (int h = 0; h < 2; ++h) { t[h] = mfma32(lds128(ap + h * 2048), pq[h][0], zero); t[h] = mfma32(lds128(ap + h * 2048 + 512), pq[h][1], t[h]); }
#pragma unroll
    for (int h = 0; h < 2; ++h) {
      const float dh = ((t[h][8] + t[h][9]) + (t[h][10] + t[h][11])) + ((t[h][12] + t[h][13]) + (t[h][14] + t[h][15]));
      const auto sw2 = __builtin_amdgcn_permlane32_swap(f2u(dh), f2u(dh), false, false);
      const float r = __builtin_amdgcn_rcpf((u2f(sw2[0]) + u2f(sw2[1])) + 1e-6f);
      st128c(rs, WS_OFR + ((4 * p + w) * 32 + 2 * j + h) * 1024 + l * 16,
             (u32x4){pk2(t[h][0] * r, t[h][1] * r), pk2(t[h][2] * r, t[h][3] * r), pk2(t[h][4] * r, t[h][5] * r), pk2(t[h][6] * r, t[h][7] * r)});
    }
    MEMB(); SCHEDB();
    lptr wb = sm + kh * 8192 + (4 * hh + q4) * 64 + blk * 32 + pp * 8;
    auto ldw = [&](int i) NOPK {
      lptr q = wb + WOB[(i >> 1) & 3] + (i >> 3) * 4096 + (i & 1) * 2048;
      wf[2 * i] = trd2(q, q + 1024); wf[2 * i + 1] = trd2(q + 512, q + 1536);
    };
#pragma unroll
    for (int i = 0; i < 16; ++i) ldw(i);
    WAITV(0);
    put_flag(rs, WS_OF + ((4 * p + w) * 16 + j) * 128, ftag, l);
  }
  BAR();
  __builtin_amdgcn_s_sleep(PRE_SLEEP);
  f32x16 acc0 = {}, acc1 = {};
  {
    const u32 obase = WS_OFR + (st * 32 + 16 * kh) * 1024 + l * 16;
    const u32 fo = WS_OF + (st * 16 + 8 * kh + (l & 7)) * 128;
    u32x4 of[16];
    wait_flags(rs, fo, ftag);
#pragma unroll
    for (int i = 0; i < 16; ++i) of[i] = ld128c(rs, obase + i * 1024);
    MEMB(); SCHEDB();
    lptr rp = sm + kh * 17408 + (32 * sh + r32) * 272 + hh * 16;
    PIN(rp);
#pragma unroll
    for (int i = 0; i < 16; ++i) {
      acc0 = mfma32(wf[2 * i], of[i], acc0);
      if (i >= 3) acc1 = mfma32(wf[2 * i - 5], of[i - 3], acc1);
      SCHEDB();
    }
#pragma unroll
    for (int i = 13; i < 16; ++i) {
      acc1 = mfma32(wf[2 * i + 1], of[i], acc1);
      if (i >= 14) {
#pragma unroll
        for (int g = 2 * (i - 14); g < 2 * (i - 14) + 2; ++g) *(LAS f32x4*)(rp + g * 32) = (f32x4){acc0[4 * g], acc0[4 * g + 1], acc0[4 * g + 2], acc0[4 * g + 3]};
      }
      SCHEDB();
    }
#pragma unroll
    for (int g = 0; g < 4; ++g) *(LAS f32x4*)(rp + 128 + g * 32) = (f32x4){acc1[4 * g], acc1[4 * g + 1], acc1[4 * g + 2], acc1[4 * g + 3]};
    BAR();
#pragma unroll
    for (int i = 0; i < 2; ++i) {
      const int row = i * 32 + (tid >> 3), oc = tid & 7;
      lptr sp = sm + row * 272 + oc * 32;
      const f32x4 a = *(LAS f32x4*)sp + *(LAS f32x4*)(sp + 17408), bb = *(LAS f32x4*)(sp + 16) + *(LAS f32x4*)(sp + 17408 + 16);
      __builtin_amdgcn_raw_buffer_store_b128((u32x4){pk2(a[0], a[1]), pk2(a[2], a[3]), pk2(bb[0], bb[1]), pk2(bb[2], bb[3])}, ro, (64 * rt + row) * 2048 + ct * 128 + oc * 16, 0, OAUX);
    }
  }
  st32c(rs, (b == 0 && tid == 0) ? WS_EPOCH : OOB, epoch + 1);
}

DI void load_u(u32 lb, const int b, const char* u) {
  const int gam = b & 7, idx = b >> 3;
  const int l = threadIdx.x & 63, w = __builtin_amdgcn_readfirstlane(threadIdx.x >> 6);
  const u32 uo = (l >> 3) * 1024 + (((l & 7) ^ (2 * (l >> 5))) * 16);
  setm0(lb + UT + w * 2048);
  const u32x4 su = mk_srd(u, 2048 * 1024);
  const u32 so = (64 * idx + 16 * w) * 1024 + gam * 128;
  dmao_nt<0>(uo, su, so); dmao_nt<1024>((uo ^ 64) + 7168, su, so);
}
DI void prologue_B(u32 lb, const int b, const char* u, const char* Are, const char* Aim, const char* Bre, const char* Bim, const char* Cre, const char* Cim) {
  const int gam = b & 7, idx = b >> 3;
  const int l = threadIdx.x & 63, w = __builtin_amdgcn_readfirstlane(threadIdx.x >> 6);
  const bool od = l & 8;
  const u64 q0 = (u64)(od ? Aim : Are), q1 = (u64)(od ? Bim : Bre), q2 = (u64)(od ? Cim : Cre);
  u64 pq_ = l < 16 ? q0 : q1; PIN(pq_);
  const u64 pa = (l < 32 ? pq_ : q2) + (u32)((64 * gam + 2 * idx) * 256 + w * 128 + (l & 7) * 16);
  asm volatile("s_mov_b32 m0, %0\n\ts_nop 0\n\tglobal_load_lds_dwordx4 %1, off nt" :: "s"(lb + RAW + w * 1024), "v"(pa) : "memory");
  load_u(lb, b, u);
}

DI void role_B(lptr sm, const int b, u32 epoch_, u32 ld2_, u32 dk2_, char* out, char* ws) {
  const int gam = b & 7, idx = b >> 3;
  const int tid = threadIdx.x;
  const int l = tid & 63, w = __builtin_amdgcn_readfirstlane(tid >> 6);
  const int g4 = l >> 4, i16 = l & 15, q4 = i16 >> 2, pp = l & 3, c = w >> 1, hs = w & 1;
  const RSRC rs = __builtin_amdgcn_make_buffer_rsrc(ws, 0, WS_END, 0x00020000);
  const RSRC ro = __builtin_amdgcn_make_buffer_rsrc(out, 0, 4u << 20, 0x00020000);
  lptr kb = sm + KL + c * KST + 40;
  lptr dummy = sm + KL + 2 * KST + l * 16;
  lptr zp = tid < 128 ? sm + UP + (tid >> 3) * UST + 16 + (tid & 7) * 16 : (tid < 136 ? sm + KL + ((tid >> 2) & 1) * KST + (tid & 3) * 16 : dummy);
  *(LAS u32x4*)zp = (u32x4){0, 0, 0, 0};
  const int beta = q4 == 0 ? pp : (q4 == 1 ? 256 + 2 * pp : (pp < 2 ? 256 * q4 + 4 * pp : 2 * q4 - 2 + pp));
  const bool first = (i16 == 0) && (hs == 0), lane0 = first && g4 == 0;
  lptr e1 = kb + (8 * i16 + 128 * hs + 256 * g4) * 2;
  lptr e2 = g4 == 0 ? dummy : e1 + 8;
  lptr e3 = g4 < 2 ? dummy : e1 - 508 * g4;
  lptr e4 = g4 < 2 ? (lane0 ? kb + 1022 * 2 : dummy) : ((first && g4 == 2) ? dummy : e1 - 4);
  lptr e5 = g4 == 1 ? e1 + 12 : ((first && g4 == 2) ? e1 - 2 : dummy);
  lptr e6 = (g4 == 1 && !first) ? e1 - 2 : dummy;
  lptr e7 = lane0 ? dummy : e1 + 2040;
  lptr e8 = lane0 ? kb + 1024 * 2 : ((l == 63 && hs == 1) ? kb + 2044 * 2 : dummy);
  PIN(e1); PIN(e2); PIN(e3); PIN(e4); PIN(e5); PIN(e6); PIN(e7); PIN(e8);
  u32 epoch, ld2, dk2;
  asm volatile("s_waitcnt vmcnt(3) lgkmcnt(0)\n\ts_mov_b32 %0, %3\n\ts_mov_b32 %1, %4\n\ts_mov_b32 %2, %5"
               : "=&s"(epoch), "=&s"(ld2), "=&s"(dk2) : "s"(epoch_), "s"(ld2_), "s"(dk2_) : "memory");
  const u32 tag = mk_tag(epoch);
  const u32x2 ftag = {tag, mk_tag2(epoch)};
  float a2, brev, ksum; f32x2 cc, m0, m1;
  {
    lptr rp = sm + RAW + w * 1024 + l * 2;
    const float are = bf1(*(LAS u16*)rp), aim = bf1(*(LAS u16*)(rp + 128)), bre = bf1(*(LAS u16*)(rp + 256)), bim = bf1(*(LAS u16*)(rp + 384));
    const float cre = bf1(*(LAS u16*)(rp + 512)), cim = bf1(*(LAS u16*)(rp + 640));
    const float ld = c ? bf_hi(ld2) : bf_lo(ld2), lt = ld * 1.4426950408889634f;
    const float lr = __builtin_fmaf(ld, 1.9259629911266175e-8f, __builtin_fmaf(ld, 1.4426950408889634f, -lt));
    const float e_ = __builtin_amdgcn_exp2f(lt);
    const float dt = rbf(__builtin_fmaf(e_ * lr, 0.6931471805599453f, e_));
    cc = cmul((f32x2){cre, cim}, (f32x2){dt * bre, dt * bim});
    a2 = (dt * are) * 1.4426950408889634f; brev = (dt * aim) * 0.15915494309189535f;
    m0 = cexp_s(a2, brev, 128.f); m1 = cexp_s(a2, brev, 1024.f);
    PIN(a2); PIN(brev); PIN(cc); PIN(m0); PIN(m1);
    const f32x2 g = cmul(cc, m1);
    ksum = __builtin_fmaf(-g.y, m1.y, g.x * m1.x);
    ksum = addd<0x111, 0xf>(ksum); ksum = addd<0x112, 0xf>(ksum); ksum = addd<0x114, 0xf>(ksum); ksum = addd<0x118, 0xf>(ksum);
    ksum = addd<0x142, 0xa>(ksum); ksum = addd<0x143, 0xc>(ksum);
    PIN(ksum);
  }
  MEMB(); SCHEDB();
  {
    f32x4 dA = {}, dB = {}, dC = {}, dD = {};
    const float sA = (float)(8 * i16), sB = (float)beta;
    const u32 ufo = uf_off(gam + 8 * (8 * w + (l & 7)), idx >> 3);
    auto kgen = [&](auto kk) NOPK {
      constexpr int ks = decltype(kk)::v;
      u32x4 o1, o2, b1, b2;
      auto elem2 = [&](auto ee) NOPK {
        constexpr int e = decltype(ee)::v, I = 4 * ks + e;
        const f32x2 ea[2] = {cexp_b<I>(a2, brev, sA), cexp_b<I + 1>(a2, brev, sA)}, eb[2] = {cexp_b<I>(a2, brev, sB), cexp_b<I + 1>(a2, brev, sB)};
        f32x2 v1[2], v2[2], w2[2];
        cprod2<I>(cc, m0, m1, ea, eb, v1, v2, w2);
#pragma unroll
        for (int k = 0; k < 2; ++k) { o1[e + k] = pk2(v1[k].x, v1[k].y); o2[e + k] = pk2(v2[k].x, v2[k].y); b1[e + k] = pk2(eb[k].x, eb[k].y); b2[e + k] = pk2(w2[k].x, w2[k].y); }
      };
      if constexpr (ks == 3) {
        u32x4 tv[2];
        WAITV(1); BAR();
#pragma unroll
        for (int e = 0; e < 2; ++e) {
          lptr a = sm + UT + (32 * (g4 >> 1) + 4 * q4 + 2 * e + (g4 & 1)) * 128 + (((2 * w + (pp >> 1)) ^ (2 * q4)) * 16) + (pp & 1) * 8;
          tv[e] = trd2(a, a + 16 * 128);
        }
#pragma unroll
        for (int e = 0; e < 2; ++e) *(LAS u32x4*)(sm + YS + (16 * w + i16) * 144 + (4 * e + 2 * (g4 & 1) + (g4 >> 1)) * 16) = tv[e];
#pragma unroll
        for (int e = 0; e < 2; ++e) tv[e] = lds128(sm + YS + (16 * w + 8 * e + (l >> 3)) * 144 + (l & 7) * 16);
        SCHEDB(); elem2(IC<0>{}); PIN(b2[1]); SCHEDB();
#pragma unroll
        for (int e = 0; e < 2; ++e) st128c(rs, WS_UX + (64 * gam + 16 * w + 8 * e + (l >> 3)) * 4096 + idx * 128 + (l & 7) * 16, tv[e]);
        SCHEDB(); elem2(IC<2>{});
      } else { elem2(IC<0>{}); elem2(IC<2>{}); }
      dA = mfma16(b1, o1, dA); dB = mfma16(b1, o2, dB); dC = mfma16(b2, o1, dC); dD = mfma16(b2, o2, dD);
      SCHEDB();
    };
    kgen(IC<0>{}); kgen(IC<1>{}); kgen(IC<2>{}); kgen(IC<3>{});
    WAITV(0); put_flag(rs, uf_off(b, w), ftag, l);
    MEMB(); SCHEDB();
    lptr xc = sm + XCH + l * 16;
    *(LAS f32x4*)(xc + w * 1024) = hs ? dA : dB;
    *(LAS f32x2*)(xc + w * 1024 + 4096) = (f32x2){hs ? dC[0] : dD[0], ksum};
    BAR();
    f32x4 dt_ = (hs ? dB : dA) + *(LAS f32x4*)(xc + (w ^ 1) * 1024);
    const f32x2 dr = (f32x2){hs ? dD[0] : dC[0], ksum} + *(LAS f32x2*)(xc + (w ^ 1) * 1024 + 4096);
    dt_[0] += lane0 ? (c ? bf_hi(dk2) : bf_lo(dk2)) : 0.f;
    {
      u32 p00 = pk2(dt_[0], dt_[0]), p01 = pk2(dt_[0], dt_[1]), p11 = pk2(dt_[1], dt_[1]), p12 = pk2(dt_[1], dt_[2]);
      u32 p22 = pk2(dt_[2], dt_[2]), p23 = pk2(dt_[2], dt_[3]);
      const float rp_ = __builtin_bit_cast(float, __builtin_amdgcn_update_dpp(0, __builtin_bit_cast(int, dr.x), 0xA0, 0xf, 0xf, true));
      u32 rr = pk2(dr.x, dr.x), rf = pk2(rp_, dr.x), kk = pk2(dr.y, dr.y);
      PIN(p00); PIN(p01); PIN(p11); PIN(p12); PIN(p22); PIN(p23); PIN(rr); PIN(kk);
      const u32 t2 = g4 == 1 ? p12 : p01, t8 = l == 0 ? rr : kk;
      *(LAS u32x4*)e7 = (u32x4){rf, rr, rr, rr};
      *(LAS u32x2*)e1 = (u32x2){g4 == 0 ? p01 : p00, g4 == 0 ? p23 : t2};
      *(LAS u32*)e2 = g4 == 1 ? p22 : p11;
      *(LAS u32*)e3 = p23;
      *(LAS u32*)e4 = g4 == 0 ? rr : p00;
      *(LAS u16*)e5 = (u16)(g4 == 1 ? p23 >> 16 : p00);
      *(LAS u16*)e6 = (u16)p00;
      *(LAS u32x2*)e8 = (u32x2){t8, t8};
    }
    MEMB(); SCHEDB();
    __builtin_amdgcn_s_sleep(UPRE);
    wait_flags(rs, ufo, ftag);
    u32x4 uv[2];
#pragma unroll
    for (int e = 0; e < 2; ++e) uv[e] = ld128c(rs, WS_UX + (64 * gam + 2 * idx + e) * 4096 + tid * 16);
#pragma unroll
    for (int e = 0; e < 2; ++e) {
      lptr q2 = sm + UP + (e * 4 + ((tid >> 1) & 3)) * UST + 128 + (tid >> 3) * 32 + (tid & 1) * 16;
      *(LAS u32x4*)q2 = uv[e];
      *(volatile LAS u32x2*)(q2 + UC2 + 8) = (u32x2){uv[e].x, uv[e].y};
      *(volatile LAS u32x2*)(q2 + UC2 + 16) = (u32x2){uv[e].z, uv[e].w};
    }
  }
  BAR();
  *(LAS f32x4*)(tid < 2 ? sm + CB + tid * CST : dummy) = (f32x4){};
  st32c(rs, (idx == 0 && tid == 0) ? WS_EPOCH + 128 * (1 + gam) : OOB, epoch + 1);
  {
    f32x4 acc[4][4];
    lptr ka = kb + (4 * pp - 4 * (8 * g4 + q4) + 112) * 2;
    lptr ub = sm + UP + c * 4 * UST + 128 + (4 * i16 + 8 * g4 - 60) * 2 + ((l & 1) ? 0 : UC2 + 8);
    if (hs == 0) conv_tiles<0, 3, 4, 7>(ka, ub, UST, acc); else conv_tiles<1, 2, 5, 6>(ka, ub, UST, acc);
    lptr cb = sm + CB + c * CST + 16;
    f32x4 y[4];
#pragma unroll
    for (int t = 0; t < 4; ++t) {
      const int tau = 2 * t + ((t & 1) ^ hs);
      const f32x4 *z = acc[t];
      *(LAS f32x4*)(cb + (64 * tau + l) * 16) = (f32x4){(z[1][3] + z[2][2]) + z[3][1], z[2][3] + z[3][2], z[3][3], z[3][3]};
    }
#pragma unroll
    for (int t = 0; t < 4; ++t) {
      const f32x4 *z = acc[t];
      y[t] = (f32x4){z[0][0], z[0][1] + z[1][0], (z[0][2] + z[1][1]) + z[2][0], (z[0][3] + z[1][2]) + (z[2][1] + z[3][0])};
      PIN(y[t]);
    }
    BAR();
    const int src = g4 > 0 ? l - 16 : (i16 > 0 ? 47 + i16 : -1);
    u32x2 yp[4];
#pragma unroll
    for (int t = 0; t < 4; ++t) {
      const int tau = 2 * t + ((t & 1) ^ hs);
      const f32x4 r = *(LAS f32x4*)(cb + (64 * tau + src) * 16);
      yp[t] = (u32x2){pk2(y[t][0] + r[0], y[t][1] + r[1]), pk2(y[t][2] + r[2], y[t][3])};
    }
#pragma unroll
    for (int t = 0; t < 4; t += 2) {
      const auto s0 = __builtin_amdgcn_permlane16_swap(yp[t].x, yp[t + 1].x, false, false);
      const auto s1 = __builtin_amdgcn_permlane16_swap(yp[t].y, yp[t + 1].y, false, false);
      const int tau = (g4 & 1) ? 2 * t + 3 - hs : 2 * t + hs;
      st128c(rs, WS_YT + (64 * gam + 2 * idx + c) * 4096 + tau * 512 + q4 * 128 + (g4 >> 1) * 64 + pp * 16, (u32x4){s0[0], s1[0], s0[1], s1[1]});
    }
  }
  MEMB(); SCHEDB();
  WAITV(0);
  put_flag(rs, WS_YF + ((gam * 4 + w) * 32 + idx) * 128, ftag, l);
  __builtin_amdgcn_s_sleep(PRE_SLEEP);
  {
    const int tl = idx >> 2, hsn = ((tl >> 1) ^ tl) & 1;
    wait_flags(rs, WS_YF + ((gam * 4 + 2 * ((l >> 3) & 1) + hsn) * 32 + 8 * w + (l & 7)) * 128, ftag);
  }
  {
    u32x4 v[2];
#pragma unroll
    for (int e = 0; e < 2; ++e) v[e] = ld128c(rs, WS_YT + (64 * gam + 16 * w + 8 * e + (l >> 3)) * 4096 + idx * 128 + (l & 7) * 16);
#pragma unroll
    for (int e = 0; e < 2; ++e) *(LAS u32x4*)(sm + YS + (16 * w + 8 * e + (l >> 3)) * 144 + (2 * pp + ((l >> 2) & 1)) * 16) = v[e];
  }
  BAR();
#pragma unroll
  for (int e = 0; e < 2; ++e) {
    lptr a = sm + YS + (8 * (4 * e + g4) + q4) * 144 + w * 32 + pp * 8;
    *(LAS u32x4*)(sm + UT + (16 * w + i16) * 144 + (4 * e + g4) * 16) = trd2(a, a + 4 * 144);
  }
#pragma unroll
  for (int e = 0; e < 2; ++e)
    __builtin_amdgcn_raw_buffer_store_b128(lds128(sm + UT + (16 * w + 8 * e + (l >> 3)) * 144 + (l & 7) * 16), ro, (64 * idx + 16 * w + 8 * e + (l >> 3)) * 2048 + 1024 + gam * 128 + (l & 7) * 16, 0, OAUX);
}

__global__ __launch_bounds__(256, 2) NOPK void fused_kernel(const char* x, const char* W, const char* wphi, const char* wo, const char* u,
                                                            const char* Are, const char* Aim, const char* Bre, const char* Bim, const char* Cre, const char* Cim,
                                                            const char* ldt, const char* Dsk, char* out, char* ws) {
  extern __shared__ __attribute__((aligned(16))) char smem[];
  lptr sm = (lptr)smem;
  asm volatile("" :: "s"(x), "s"(W), "s"(wphi), "s"(wo), "s"(u), "s"(Are), "s"(Aim), "s"(Bre), "s"(Bim), "s"(Cre), "s"(Cim), "s"(ldt), "s"(Dsk), "s"(out), "s"(ws));
  const int b = blockIdx.x & 255;
  u32 isA = blockIdx.x >> 8;
  u32 epoch_, ld2_, dk2_;
  asm volatile("s_load_dword %0, %3, %4\n\ts_load_dword %1, %5, %7\n\ts_load_dword %2, %6, %7"
               : "=&s"(epoch_), "=&s"(ld2_), "=&s"(dk2_) : "s"(ws), "s"(isA ? WS_EPOCH : WS_EPOCH + 128 * (1 + (b & 7))), "s"(ldt), "s"(Dsk), "s"((64 * (b & 7) + 2 * (b >> 3)) * 2) : "memory");
  __builtin_amdgcn_s_setprio(3);
  if (__builtin_expect(!isA, 1)) {
    {
      u64 pc; asm volatile("s_getpc_b64 %0" : "=s"(pc));
      const u32 ln = (b >> 3) * 8 + (threadIdx.x >> 5);
      asm volatile("s_mov_b32 m0, %0\n\ts_nop 0\n\tglobal_load_lds_dword %1, %2" :: "s"((u32)(size_t)sm + 81408), "v"((ln < CODE_LINES ? ln : 0u) * 128u), "s"(pc & ~127ull) : "memory");
    }
    prologue_B((u32)(size_t)sm, b, u, Are, Aim, Bre, Bim, Cre, Cim);
    {
      const int xcd = b & 7, idx = b >> 3, w = threadIdx.x >> 6, k = threadIdx.x & 3;
      const int p = (xcd >> 1) * 4 + (idx >> 3), j = (xcd & 1) * 8 + (idx & 7);
      const u32 o = k == 0 ? uf_off(b, w) : (k == 1 ? WS_YF + ((xcd * 4 + w) * 32 + idx) * 128 : (k == 2 ? WS_KVF + ((j * 4 + w) * 16 + p) * 128 : WS_OF + ((4 * p + w) * 16 + j) * 128));
      asm volatile("s_mov_b32 m0, %0\n\ts_nop 0\n\tglobal_load_lds_dword %1, %2 sc1" :: "s"((u32)(size_t)sm + 81408), "v"(o), "s"(ws) : "memory");
    }
    __builtin_amdgcn_s_setprio(0); role_B(sm, b, epoch_, ld2_, dk2_, out, ws);
  } else { __builtin_amdgcn_s_setprio(2); role_A(sm, b, epoch_, ld2_, dk2_, x, W, wphi, wo, out, ws); }
}

extern "C" void kernel_launch(void* const* d_in, const int* in_sizes, int n_in, void* d_out, int out_size, void* d_ws, size_t ws_size,
                              hipStream_t stream) {
  (void)hipFuncSetAttribute((const void*)fused_kernel, hipFuncAttributeMaxDynamicSharedMemorySize, LDS_BYTES);
  fused_kernel<<<512, 256, LDS_BYTES, stream>>>((const char*)d_in[0], (const char*)d_in[1], (const char*)d_in[2], (const char*)d_in[3], (const char*)d_in[4],
                                                (const char*)d_in[5], (const char*)d_in[6], (const char*)d_in[7], (const char*)d_in[8], (const char*)d_in[9], (const char*)d_in[10],
                                                (const char*)d_in[11], (const char*)d_in[12], (char*)d_out, (char*)d_ws);
}
```
